```python
import torch

def _softmax(x, axis=-1):
    return torch.softmax(x, dim=axis)
def _gelu(x):
    return torch.nn.functional.gelu(x, approximate="tanh")
def _silu(x):
    return torch.nn.functional.silu(x)
def _sigmoid(x):
    return torch.sigmoid(x)
def _layernorm(x, axis=-1, eps=1e-5):
    m = x.mean(dim=axis, keepdim=True)
    v = x.var(dim=axis, keepdim=True, unbiased=False)
    return (x - m) / torch.sqrt(v + eps)
def _scatter_add(tgt, idx, src, axis=0):
    return tgt.clone().index_add_(axis, idx, src)
def _segment_sum(src, seg, n):
    out = torch.zeros((n,) + src.shape[1:], dtype=src.dtype)
    return out.index_add_(0, seg, src)
def _pad(x, pw):
    flat = tuple(v for pair in reversed(pw) for v in pair)
    return torch.nn.functional.pad(x, flat)

kernel_name = 'parallel_sum__linear_attn_block__dense_activation_squared_relu__prompt'

def setup_inputs():

    _w = (lambda a: (lambda t: t.to(torch.bfloat16) if t.is_floating_point() else t)(torch.as_tensor(a)))
    def _A_setup():
        B, S, D = 1, 2048, 576
        return {
            "x": _w((lambda *s: torch.randn(*s, dtype=torch.bfloat16) * 0.05)(B, S, D)),
            "wqkv": _w((lambda *s: torch.randn(*s, dtype=torch.bfloat16) * 0.4)(D, 3 * D)),
            "wo": _w((lambda *s: torch.randn(*s, dtype=torch.bfloat16) * 0.4)(D, D)),
        }
    _A_in = _A_setup()

    def _B_setup():
        B, S, D, F = 1, 2048, 576, 4608
        return {
            "x": _w((lambda *s: torch.randn(*s, dtype=torch.bfloat16) * 0.05)(B, S, D)),
            "w1": _w((lambda *s: torch.randn(*s, dtype=torch.bfloat16) * 0.05)(D, F)),
            "b1": _w((lambda *s: torch.randn(*s, dtype=torch.bfloat16) * 0.2)(F)),
            "w2": _w((lambda *s: torch.randn(*s, dtype=torch.bfloat16) * 0.05)(F, D)),
            "b2": _w((lambda *s: torch.randn(*s, dtype=torch.bfloat16) * 1.6)(D)),
        }
    _B_in = _B_setup()

    return {
        "A_x": _A_in["x"],
        "A_wqkv": _A_in["wqkv"],
        "A_wo": _A_in["wo"],
        "B_x": _B_in["x"],
        "B_w1": _B_in["w1"],
        "B_b1": _B_in["b1"],
        "B_w2": _B_in["w2"],
        "B_b2": _B_in["b2"],
    }

def reference(A_x, A_wqkv, A_wo, B_x, B_w1, B_b1, B_w2, B_b2):
    def _A_ref(x, wqkv, wo):
        B, S, D = x.shape
        H = 32; Dh = D // H
        qkv = (lambda x, *d: x.permute(*d))((x @ wqkv).reshape(B, S, 3, H, Dh), 2, 0, 3, 1, 4)
        q, k, v = qkv[0], qkv[1], qkv[2]
        phi = lambda t: torch.where(t > 0, t + 1, torch.exp(t)) + 1e-6
        pq, pk = phi(q), phi(k)
        kv = torch.einsum("bhsd,bhse->bhde", pk, v)
        z = torch.einsum("bhsd->bhd", pk)
        num = torch.einsum("bhqd,bhde->bhqe", pq, kv)
        den = torch.einsum("bhqd,bhd->bhq", pq, z)[..., None] + 1e-6
        o = num / den
        return (lambda x, *d: x.permute(*d))(o, 0, 2, 1, 3).reshape(B, S, D) @ wo

    def _B_ref(x, w1, b1, w2, b2):
        h = torch.einsum("bsd,df->bsf", x, w1) + b1
        zero = torch.zeros_like(h)
        r = torch.where(h > zero, h, zero)
        h = r * r
        out = torch.einsum("bsf,fd->bsd", h, w2) + b2
        return out

    x = A_x
    xn = _layernorm(x, axis=-1)
    return x + _A_ref(xn, A_wqkv, A_wo) + _B_ref(xn, B_w1, B_b1, B_w2, B_b2)
```

```cpp
#include <hip/hip_runtime.h>
#include <hip/hip_bf16.h>

typedef __attribute__((ext_vector_type(4))) short s16x4;
typedef __attribute__((ext_vector_type(8))) __bf16 bf16x8;
typedef __attribute__((ext_vector_type(2))) __bf16 bf16x2;
typedef __attribute__((ext_vector_type(4))) float f32x4;
typedef __attribute__((ext_vector_type(2))) float f32x2;
typedef __attribute__((ext_vector_type(4))) unsigned u32x4;
typedef __attribute__((ext_vector_type(2))) unsigned u32x2;
#define LAS __attribute__((address_space(3)))
#define GAS __attribute__((address_space(1)))
#define DI __device__ __forceinline__
#define SCHED() __builtin_amdgcn_sched_barrier(0)
#define BAR() __builtin_amdgcn_s_barrier()
#define WAIT_VM(n) asm volatile("s_waitcnt vmcnt(%0)" ::"n"(n) : "memory")
#define WAIT_LGKM(n) asm volatile("s_waitcnt lgkmcnt(%0)" ::"n"(n) : "memory")

namespace {

constexpr int DM = 576, FF = 4608;
constexpr int ACT_STRIDE = 1184;
constexpr int RING = 64 * ACT_STRIDE;
constexpr int KSLOT = 36864;
constexpr int PQ = RING + 2 * KSLOT;
constexpr int PQ_STRIDE = 144;
constexpr int SIMG = PQ + 64 * PQ_STRIDE;
constexpr int RED = SIMG + 2752;
constexpr int LDS_BYTES = 163840;
constexpr int KTMP = RING + 16384;
constexpr int VTMP = KTMP + 64 * 144;
static_assert(RED + 2048 <= LDS_BYTES, "lds");
static_assert(VTMP + 64 * 152 + 16 <= RING + KSLOT, "lds");

constexpr unsigned WS_EPOCH = 0;
constexpr unsigned WS_FLAG1 = 1024;
constexpr unsigned WS_FLAG2 = 2048;
constexpr unsigned WS_FLAG3 = 4096;
constexpr unsigned WS_P = 65536;
constexpr unsigned WS_S = WS_P + 256 * 16384;
constexpr unsigned WS_SLAB = WS_S + 8 * 16384;
constexpr unsigned WS_END = WS_SLAB + 32 * 8 * 8 * 9216;
constexpr int AUX_SC1 = 16;
constexpr unsigned SPIN_CAP = 1u << 16;

DI float bf_lo(unsigned u) { return __builtin_bit_cast(float, u << 16); }
DI float bf_hi(unsigned u) { return __builtin_bit_cast(float, u & 0xffff0000u); }
DI unsigned pk2(float a, float b) {
  f32x2 p = {a, b};
  return __builtin_bit_cast(unsigned, __builtin_convertvector(p, bf16x2));
}
DI float rbf(float a) { return bf_lo(pk2(a, a)); }
template <int CTRL> DI float dpp_add(float v) {
  return v + __builtin_bit_cast(float, __builtin_amdgcn_update_dpp(0, __builtin_bit_cast(int, v), CTRL, 0xf, 0xf, true));
}
DI int fresh_lane() { int v = threadIdx.x & 63; asm volatile("" : "+v"(v)); return v; }
DI bf16x8 lds_b128(unsigned addr) { return *(LAS bf16x8*)(size_t)addr; }
DI s16x4 lds_tr(unsigned addr) { return __builtin_amdgcn_ds_read_tr16_b64_v4i16((LAS s16x4*)(size_t)addr); }
DI bf16x8 join(s16x4 lo, s16x4 hi) {
  return __builtin_bit_cast(bf16x8, __builtin_shufflevector(lo, hi, 0, 1, 2, 3, 4, 5, 6, 7));
}
DI f32x4 mfma(bf16x8 a, bf16x8 b, f32x4 c) { return __builtin_amdgcn_mfma_f32_16x16x32_bf16(a, b, c, 0, 0, 0); }
DI float phi(float v) {
  float e = __builtin_amdgcn_exp2f(v * 1.44269504088896f);
  return (v > 0.f ? v + 1.f : e) + 1e-6f;
}
DI unsigned ld_flag(const unsigned* p) { return __hip_atomic_load((GAS unsigned*)p, __ATOMIC_RELAXED, __HIP_MEMORY_SCOPE_AGENT); }
DI void st_flag(unsigned* p, unsigned v) { __hip_atomic_store((GAS unsigned*)p, v, __ATOMIC_RELAXED, __HIP_MEMORY_SCOPE_AGENT); }
DI void wait_flags(const unsigned* base, int count, unsigned tag, int lane, unsigned first) {
  unsigned v = first;
  for (unsigned spins = 0; spins < SPIN_CAP; ++spins) {
    if (__all(lane >= count || v == tag)) break;
    __builtin_amdgcn_s_sleep(2);
    if (lane < count) v = ld_flag(base + lane);
  }
  asm volatile("" ::: "memory");
}

template <int IMM> DI void dma(unsigned voff, const char* sbase, unsigned m0v) {
  asm volatile("s_mov_b32 m0, %1\n\ts_nop 0\n\tglobal_load_lds_dwordx4 %0, %2 offset:%3" ::"v"(voff), "s"(m0v), "s"(sbase), "n"(IMM) : "memory");
}
template <int B0, int NB> DI void dma_blocks(unsigned voff, const char* sbase, unsigned m0base) {
#pragma unroll
  for (int b = B0; b < B0 + NB; ++b) {
    if (b == 0) dma<0>(voff, sbase, m0base);
    if (b == 1) dma<128>(voff, sbase, m0base + 1 * (4096 - 128));
    if (b == 2) dma<256>(voff, sbase, m0base + 2 * (4096 - 128));
    if (b == 3) dma<384>(voff, sbase, m0base + 3 * (4096 - 128));
    if (b == 4) dma<512>(voff, sbase, m0base + 4 * (4096 - 128));
    if (b == 5) dma<640>(voff, sbase, m0base + 5 * (4096 - 128));
    if (b == 6) dma<768>(voff, sbase, m0base + 6 * (4096 - 128));
    if (b == 7) dma<896>(voff, sbase, m0base + 7 * (4096 - 128));
    if (b == 8) dma<1024>(voff, sbase, m0base + 8 * (4096 - 128));
  }
}

template <int BOFF0>
DI void kstep9(f32x4 (&acc)[9][4], bf16x8 (&A)[9], const bf16x8 (&Bc)[4], bf16x8 (&Bn)[4], unsigned ab, unsigned abn, unsigned bn) {
  s16x4 lx, ly;
  constexpr int BTS = 16 * ACT_STRIDE;
#define MM(blk, j) acc[blk][j] = mfma(A[blk], Bc[j], acc[blk][j])
#define RL(v, base, blk) v = lds_tr((base) + (blk) * 4096)
#define RH(v, base, blk) A[blk] = join(v, lds_tr((base) + (blk) * 4096 + 512))
  RL(lx, ab, 3); SCHED(); MM(0, 0); SCHED(); RH(lx, ab, 3); SCHED(); MM(0, 1); SCHED(); MM(0, 2); MM(0, 3); SCHED();
  MM(1, 0); SCHED(); Bn[0] = lds_b128(bn + BOFF0); SCHED(); MM(1, 1); SCHED();
  Bn[1] = lds_b128(bn + BOFF0 + BTS); SCHED(); MM(1, 2); MM(1, 3); SCHED();
  WAIT_LGKM(2);
  BAR();
  RL(lx, ab, 4); SCHED(); MM(2, 0); SCHED(); RH(lx, ab, 4); SCHED(); MM(2, 1); SCHED();
  RL(ly, ab, 5); SCHED(); MM(2, 2); SCHED(); RH(ly, ab, 5); SCHED(); MM(2, 3); SCHED();
  RL(lx, ab, 6); SCHED(); MM(3, 0); SCHED(); RH(lx, ab, 6); SCHED(); MM(3, 1); SCHED(); MM(3, 2); MM(3, 3); SCHED();
  RL(lx, ab, 7); SCHED(); MM(4, 0); SCHED(); RH(lx, ab, 7); SCHED(); MM(4, 1); SCHED(); MM(4, 2); MM(4, 3); SCHED();
  RL(lx, ab, 8); SCHED(); MM(5, 0); SCHED(); RH(lx, ab, 8); SCHED(); MM(5, 1); SCHED(); MM(5, 2); MM(5, 3); SCHED();
  MM(6, 0); SCHED(); Bn[2] = lds_b128(bn + BOFF0 + 2 * BTS); SCHED(); MM(6, 1); SCHED();
  Bn[3] = lds_b128(bn + BOFF0 + 3 * BTS); SCHED(); MM(6, 2); MM(6, 3); SCHED();
  WAIT_LGKM(2);
  BAR();
  RL(lx, abn, 0); SCHED(); MM(7, 0); SCHED(); RH(lx, abn, 0); SCHED(); MM(7, 1); SCHED();
  RL(ly, abn, 1); SCHED(); MM(7, 2); SCHED(); RH(ly, abn, 1); SCHED(); MM(7, 3); SCHED();
  RL(lx, abn, 2); SCHED(); MM(8, 0); SCHED(); RH(lx, abn, 2); SCHED(); MM(8, 1); SCHED(); MM(8, 2); MM(8, 3); SCHED();
}
template <int BOFF0>
DI void kstep4(f32x4 (&acc)[4][4], bf16x8 (&A)[4], const bf16x8 (&Bc)[4], bf16x8 (&Bn)[4], unsigned ab, unsigned abn, unsigned bn) {
  s16x4 lx, ly;
  constexpr int BTS = 16 * ACT_STRIDE;
  RL(lx, ab, 3); SCHED(); MM(0, 0); SCHED(); RH(lx, ab, 3); SCHED(); MM(0, 1); SCHED(); MM(0, 2); MM(0, 3); SCHED();
  MM(1, 0); SCHED(); Bn[0] = lds_b128(bn + BOFF0); SCHED(); MM(1, 1); SCHED();
  Bn[1] = lds_b128(bn + BOFF0 + BTS); SCHED(); MM(1, 2); MM(1, 3); SCHED();
  WAIT_LGKM(2);
  BAR();
  RL(lx, abn, 0); SCHED(); MM(2, 0); SCHED(); RH(lx, abn, 0); SCHED(); MM(2, 1); SCHED();
  RL(ly, abn, 1); SCHED(); MM(2, 2); SCHED(); RH(ly, abn, 1); SCHED(); MM(2, 3); SCHED();
  RL(lx, abn, 2); SCHED(); MM(3, 0); SCHED(); RH(lx, abn, 2); SCHED();
  Bn[2] = lds_b128(bn + BOFF0 + 2 * BTS); SCHED(); MM(3, 1); SCHED();
  Bn[3] = lds_b128(bn + BOFF0 + 3 * BTS); SCHED(); MM(3, 2); MM(3, 3); SCHED();
}
#undef MM

}

__global__ __launch_bounds__(512, 2) void fused_block_kernel(
    const unsigned short* __restrict__ x, const char* __restrict__ wqkv, const char* __restrict__ wo,
    const char* __restrict__ w1, const unsigned short* __restrict__ b1, const char* __restrict__ w2,
    const unsigned short* __restrict__ b2, unsigned short* __restrict__ out, char* __restrict__ ws) {
  extern __shared__ __attribute__((aligned(16))) char lds[];
  const int tid = threadIdx.x, lane = tid & 63;
  const int wid = __builtin_amdgcn_readfirstlane(tid >> 6);
  const int g = blockIdx.x & 7, t = blockIdx.x >> 3;
  const unsigned LB = (unsigned)(size_t)lds;
  const __amdgpu_buffer_rsrc_t wsr = __builtin_amdgcn_make_buffer_rsrc(ws, 0, WS_END, 0x00020000);

  const int lr = lane >> 3, lc = lane & 7;
  u32x4 xr[9];
  {
    const char* xp = (const char*)x + (size_t)(t * 64 + wid * 8) * 1152;
    const unsigned xo = lr * 1152 + lc * 16;
#pragma unroll
    for (int i = 0; i < 9; ++i)
      asm volatile("global_load_dwordx4 %0, %1, %2 offset:%3" : "=v"(xr[i]) : "v"(xo), "s"(xp), "n"(i * 128) : "memory");
  }
  unsigned epoch;
  asm volatile("global_load_dword %0, %1, %2 sc1" : "=v"(epoch) : "v"(0u), "s"(ws) : "memory");

  const int l = wid - 4;
  const int lf = ((lr >> 1) & 1) << 1 | ((l & 1) << 2);
  const int lcc = lc ^ lf;
  const unsigned voffQ = lr * 3456 + lcc * 16;
  const unsigned voffQ3 = lr * 3456 + (lcc == 1 ? 1152 + 128 : lcc == 2 ? 2304 + 128 : 128);
  const char* baseQ = wqkv + g * 144 + (size_t)(8 * l) * 3456;
  const unsigned m0l = LB + RING + l * 1024;
  auto issueQ = [&](int j) {
    const char* sb = baseQ + (size_t)j * 32 * 3456;
    const unsigned m = m0l + (j & 1) * 16384 + ((j >> 1) & 1) * KSLOT;
    dma<0>(voffQ, sb, m);
    dma<1152>(voffQ, sb, m + 4096 - 1152);
    dma<2304>(voffQ, sb, m + 8192 - 2304);
    dma<0>(voffQ3, sb, m + 12288);
  };
  if (wid >= 4) {
    issueQ(0); issueQ(1); issueQ(2);
    WAIT_VM(12);
  } else {
    WAIT_VM(0);
  }
  SCHED();
#pragma unroll
  for (int i = 0; i < 9; ++i) asm volatile("" : "+v"(xr[i]));
  asm volatile("" : "+v"(epoch));
  const unsigned tag = __builtin_amdgcn_readfirstlane(epoch) + 1u;

  {
    const bf16x2 ones = __builtin_bit_cast(bf16x2, 0x3f803f80u);
    float s0 = 0.f, s1 = 0.f, q0 = 0.f, q1 = 0.f;
#pragma unroll
    for (int i = 0; i < 9; ++i) {
      const unsigned u0 = xr[i][0], u1 = xr[i][1], u2 = xr[i][2], u3 = xr[i][3];
      const bf16x2 a = __builtin_bit_cast(bf16x2, u0), b = __builtin_bit_cast(bf16x2, u1);
      const bf16x2 c = __builtin_bit_cast(bf16x2, u2), d = __builtin_bit_cast(bf16x2, u3);
      s0 = __builtin_amdgcn_fdot2_f32_bf16(a, ones, s0, false); s1 = __builtin_amdgcn_fdot2_f32_bf16(b, ones, s1, false);
      s0 = __builtin_amdgcn_fdot2_f32_bf16(c, ones, s0, false); s1 = __builtin_amdgcn_fdot2_f32_bf16(d, ones, s1, false);
      q0 = __builtin_amdgcn_fdot2_f32_bf16(a, a, q0, false); q1 = __builtin_amdgcn_fdot2_f32_bf16(b, b, q1, false);
      q0 = __builtin_amdgcn_fdot2_f32_bf16(c, c, q0, false); q1 = __builtin_amdgcn_fdot2_f32_bf16(d, d, q1, false);
    }
    float s = s0 + s1, q = q0 + q1;
    s = dpp_add<0xB1>(s); q = dpp_add<0xB1>(q);
    s = dpp_add<0x4E>(s); q = dpp_add<0x4E>(q);
    s = dpp_add<0x141>(s); q = dpp_add<0x141>(q);
    const float mean = s * (1.f / 576.f);
    const float var = fmaxf(q * (1.f / 576.f) - mean * mean, 0.f);
    const float mb = rbf(mean);
    const float sd = rbf(__builtin_sqrtf(rbf(rbf(var) + 1e-5f)));
    const float rs = 1.f / sd, nb = -mb * rs;
    const unsigned wa = LB + (wid * 8 + lr) * ACT_STRIDE + lc * 16;
#pragma unroll
    for (int i = 0; i < 9; ++i) {
      u32x4 o;
#pragma unroll
      for (int e = 0; e < 4; ++e) o[e] = pk2(fmaf(bf_lo(xr[i][e]), rs, nb), fmaf(bf_hi(xr[i][e]), rs, nb));
      *(LAS u32x4*)(size_t)(wa + i * 128) = o;
    }
  }

  if (wid >= 4) {
    __builtin_amdgcn_s_setprio(2);
    const unsigned voff1 = lr * 9216 + lcc * 16, voff2 = lr * 1152 + lcc * 16;
    const char* base1 = w1 + g * 1152 + (size_t)(8 * l) * 9216;
    const char* base2 = w2 + (size_t)(g * 576 + 8 * l) * 1152;
    const char* baseO = wo + (size_t)(g * 72) * 1152;
    auto issue1 = [&](int kk, int second) {
      const char* sb = base1 + (size_t)kk * 32 * 9216;
      const unsigned m = m0l + ((kk + 1) & 1) * KSLOT;
      if (second) dma_blocks<4, 5>(voff1, sb, m); else dma_blocks<0, 4>(voff1, sb, m);
    };
    auto issue2 = [&](int kk, int second) {
      const char* sb = kk < 18 ? base2 + (size_t)kk * 32 * 1152
                               : (kk < 20 ? baseO + (size_t)((kk - 18) * 32 + 8 * l) * 1152 : baseO + (size_t)64 * 1152);
      const unsigned m = m0l + ((kk + 1) & 1) * KSLOT;
      if (second) dma_blocks<4, 5>(voff2, sb, m); else dma_blocks<0, 4>(voff2, sb, m);
    };
    WAIT_LGKM(0);
    WAIT_VM(8);
    BAR();
    for (int j = 0; j < 15; ++j) {
      issueQ(j + 3);
      WAIT_VM(8);
      BAR();
    }
    issue1(0, 0); WAIT_VM(8); BAR();
    issue1(0, 1); WAIT_VM(9); BAR();
    issue1(1, 0);
    BAR();
    BAR();
    WAIT_VM(9);
    BAR();
    for (int kk = 0; kk < 16; ++kk) {
      issue1(kk + 1, 1); WAIT_VM(9); BAR();
      issue1(kk + 2, 0); WAIT_VM(9); BAR();
    }
    issue1(17, 1); WAIT_VM(9); BAR();
    issue2(0, 0); WAIT_VM(9); BAR();
    issue2(0, 1); WAIT_VM(9); BAR();
    issue2(1, 0); WAIT_VM(9); BAR();
    issue2(1, 1);
    BAR();
    WAIT_VM(9); BAR();
    issue2(2, 0); WAIT_VM(9); BAR();
    const int hl = l;
    bf16x8 ka0, ka1;
    u32x4 pb[4];
    f32x4 num0[4], num1[4];
    for (int kk = 1; kk < 19; ++kk) {
      issue2(kk + 1, 1); WAIT_VM(9); BAR();
      if (kk == 12) {
        const int lane = fresh_lane();
        const unsigned sa = LB + SIMG + hl * 684 + (lane & 15) * 36 + (lane >> 4) * 16;
        const int g4 = lane >> 4;
        u32x4 a0, a1;
#pragma unroll
        for (int m = 0; m < 4; ++m) {
          const bool kv = 8 * g4 + 2 * m < 18;
          const unsigned v0 = *(LAS unsigned*)(size_t)(kv ? sa + 4 * m : LB + SIMG);
          const unsigned v1 = *(LAS unsigned*)(size_t)((kv && (lane & 15) < 3) ? sa + 16 * 36 + 4 * m : LB + SIMG);
          a0[m] = kv ? v0 : 0u;
          a1[m] = (kv && (lane & 15) < 3) ? v1 : 0u;
        }
        ka0 = __builtin_bit_cast(bf16x8, a0);
        ka1 = __builtin_bit_cast(bf16x8, a1);
        const unsigned pa = LB + PQ + (lane & 15) * PQ_STRIDE + hl * 36 + (g4 < 3 ? g4 * 16 : 0);
#pragma unroll
        for (int tt = 0; tt < 4; ++tt)
#pragma unroll
          for (int m = 0; m < 4; ++m) {
            const bool kv = 8 * g4 + 2 * m < 18;
            const unsigned v = *(LAS unsigned*)(size_t)(pa + tt * 16 * PQ_STRIDE + (kv ? 4 * m : 0));
            pb[tt][m] = kv ? v : 0u;
          }
      }
      if (kk == 13) {
#pragma unroll
        for (int tt = 0; tt < 4; ++tt) {
          const bf16x8 b = __builtin_bit_cast(bf16x8, pb[tt]);
          num0[tt] = mfma(ka0, b, (f32x4){0.f, 0.f, 0.f, 0.f});
          num1[tt] = mfma(ka1, b, (f32x4){0.f, 0.f, 0.f, 0.f});
        }
      }
      if (kk == 14) {
        const int lane = fresh_lane();
        const int g4 = lane >> 4;
        const unsigned oa = LB + (lane & 15) * ACT_STRIDE + hl * 36;
#pragma unroll
        for (int tt = 0; tt < 4; ++tt) {
          const float den = rbf(rbf(__shfl(num1[tt][2], lane & 15)) + 1e-6f);
          const float r = 1.f / den;
          const unsigned p01 = pk2(rbf(num0[tt][0]) * r, rbf(num0[tt][1]) * r), p23 = pk2(rbf(num0[tt][2]) * r, rbf(num0[tt][3]) * r);
          const unsigned o = oa + tt * 16 * ACT_STRIDE;
          *(LAS unsigned*)(size_t)(o + g4 * 8) = p01;
          *(LAS unsigned*)(size_t)(o + g4 * 8 + 4) = p23;
          if (g4 == 0) *(LAS unsigned*)(size_t)(o + 32) = pk2(rbf(num1[tt][0]) * r, rbf(num1[tt][1]) * r);
          if (g4 != 0) *(LAS unsigned*)(size_t)(LB + (16 * tt + (lane & 15)) * ACT_STRIDE + 144 + hl * 12 + (g4 - 1) * 4) = 0u;
        }
        WAIT_LGKM(0);
      }
      issue2(kk + 2, 0); WAIT_VM(9); BAR();
    }
    issue2(20, 1); WAIT_VM(9); BAR();
    WAIT_VM(5); BAR();
    WAIT_VM(0); BAR();
    BAR();
  } else {
    const int w = wid;
    const int g4 = lane >> 4, q = (lane & 15) >> 2, p = lane & 3;
    const int cf = (((q >> 1) & 1) << 1) | ((g4 & 1) << 2);
    const unsigned a0 = LB + RING + (8 * g4 + q) * 128 + (((2 * w + (p >> 1)) ^ cf) * 16) + (p & 1) * 8;
    const unsigned a1 = a0 + KSLOT;
    const unsigned b0 = LB + (lane & 15) * ACT_STRIDE + g4 * 16;
    bf16x8 B0[4], B1[4];
    f32x4 bias[9];
    {
      f32x4 qa[4][4];
      bf16x8 A[4];
#pragma unroll
      for (int i = 0; i < 4; ++i)
#pragma unroll
        for (int j = 0; j < 4; ++j) qa[i][j] = (f32x4){0.f, 0.f, 0.f, 0.f};
      WAIT_LGKM(0);
      BAR();
#pragma unroll
      for (int j = 0; j < 4; ++j) B0[j] = lds_b128(b0 + j * 16 * ACT_STRIDE);
      A[0] = join(lds_tr(a0), lds_tr(a0 + 512));
      A[1] = join(lds_tr(a0 + 4096), lds_tr(a0 + 4096 + 512));
      A[2] = join(lds_tr(a0 + 8192), lds_tr(a0 + 8192 + 512));
      unsigned ac = a0, an = a1, bc = b0;
#pragma unroll 1
      for (int it = 0; it < 9; ++it) {
        kstep4<64>(qa, A, B0, B1, ac, ac + 16384, bc);
        kstep4<128>(qa, A, B1, B0, ac + 16384, an, bc);
        const unsigned tmp = ac; ac = an; an = tmp;
        bc += 128;
      }
      const int lane = fresh_lane(), g4 = lane >> 4, q = (lane & 15) >> 2, p = lane & 3;
#pragma unroll
      for (int blk = 0; blk < 9; ++blk) {
        const u32x2 bb = *(const u32x2*)(b1 + g * 576 + 64 * blk + 16 * w + 4 * g4);
        bias[blk] = (f32x4){bf_lo(bb[0]), bf_hi(bb[0]), bf_lo(bb[1]), bf_hi(bb[1])};
      }
#pragma unroll
      for (int tt = 0; tt < 4; ++tt) {
        const int tok = 16 * tt + (lane & 15);
        u32x2 o;
        o[0] = pk2(phi(qa[0][tt][0]), phi(qa[0][tt][1])); o[1] = pk2(phi(qa[0][tt][2]), phi(qa[0][tt][3]));
        *(LAS u32x2*)(size_t)(LB + PQ + tok * PQ_STRIDE + (16 * w + 4 * g4) * 2) = o;
        o[0] = pk2(phi(qa[1][tt][0]), phi(qa[1][tt][1])); o[1] = pk2(phi(qa[1][tt][2]), phi(qa[1][tt][3]));
        *(LAS u32x2*)(size_t)(LB + KTMP + tok * 144 + (16 * w + 4 * g4) * 2) = o;
        o[0] = pk2(qa[2][tt][0], qa[2][tt][1]); o[1] = pk2(qa[2][tt][2], qa[2][tt][3]);
        *(LAS u32x2*)(size_t)(LB + VTMP + tok * 152 + (16 * w + 4 * g4) * 2) = o;
        if (w == 0) {
          o[0] = pk2(phi(qa[3][tt][0]), phi(qa[3][tt][1])); o[1] = pk2(phi(qa[3][tt][2]), phi(qa[3][tt][3]));
          const unsigned dst = g4 < 2 ? LB + PQ + tok * PQ_STRIDE + 128 + g4 * 8 : LB + KTMP + tok * 144 + 128 + (g4 - 2) * 8;
          *(LAS u32x2*)(size_t)dst = o;
        } else if (w == 1) {
          o[0] = pk2(qa[3][tt][0], qa[3][tt][1]); o[1] = pk2(qa[3][tt][2], qa[3][tt][3]);
          if (g4 == 2) { o[0] = 0x00003f80u; o[1] = 0u; }
          if (g4 < 3) *(LAS u32x2*)(size_t)(LB + VTMP + tok * 152 + 128 + g4 * 8) = o;
        }
      }
      WAIT_LGKM(0);
      BAR();
      const unsigned ti = (unsigned)(0x4433322221111000ull >> (16 * w)) & 0xffffu;
      const unsigned tj = (unsigned)(0x4343243214210410ull >> (16 * w)) & 0xffffu;
      const unsigned kr = LB + KTMP + (8 * g4 + q) * 144 + p * 8, vr = LB + VTMP + (8 * g4 + q) * 152 + p * 8;
#pragma unroll
      for (int n = 0; n < 4; ++n) {
        const unsigned di = (ti >> (4 * n)) & 15u, ej = (tj >> (4 * n)) & 15u;
        f32x4 d = {0.f, 0.f, 0.f, 0.f};
#pragma unroll
        for (int ks = 0; ks < 2; ++ks) {
          const unsigned ka = kr + ks * 32 * 144 + di * 32, va = vr + ks * 32 * 152 + ej * 32;
          const bf16x8 fa = join(lds_tr(ka), lds_tr(ka + 4 * 144));
          const bf16x8 fb = join(lds_tr(va), lds_tr(va + 4 * 152));
          d = mfma(fa, fb, d);
        }
        __builtin_amdgcn_raw_buffer_store_b128(__builtin_bit_cast(u32x4, d), wsr,
                                               WS_P + (g * 32 + t) * 16384 + (4 * w + n) * 1024 + lane * 16, 0, AUX_SC1);
      }
      WAIT_LGKM(0);
      BAR();
    }
    f32x4 acc[9][4];
    bf16x8 A[9];
#pragma unroll
    for (int i = 0; i < 9; ++i)
#pragma unroll
      for (int j = 0; j < 4; ++j) acc[i][j] = bias[i];
#pragma unroll
    for (int j = 0; j < 4; ++j) B0[j] = lds_b128(b0 + j * 16 * ACT_STRIDE);
    A[0] = join(lds_tr(a1), lds_tr(a1 + 512));
    A[1] = join(lds_tr(a1 + 4096), lds_tr(a1 + 4096 + 512));
    A[2] = join(lds_tr(a1 + 8192), lds_tr(a1 + 8192 + 512));
    unsigned bc = b0;
    unsigned fl = 0;
    u32x4 pl[4];
#pragma unroll 1
    for (int it = 0; it < 9; ++it) {
      if (it == 1) WAIT_VM(0);
      const int lane = fresh_lane();
      if (it == 2 && w == 0 && lane == 0) st_flag((unsigned*)(ws + WS_FLAG1) + g * 32 + t, tag);
      if (it == 5) fl = lane < 32 ? ld_flag((const unsigned*)(ws + WS_FLAG1) + g * 32 + lane) : tag;
      if (it == 6) {
        wait_flags((const unsigned*)(ws + WS_FLAG1) + g * 32, 32, tag, lane, fl);
#pragma unroll
        for (int n = 0; n < 4; ++n)
          pl[n] = __builtin_amdgcn_raw_buffer_load_b128(wsr, WS_P + (g * 32 + 8 * w + 2 * n + (lane >> 5)) * 16384 + t * 512 + (lane & 31) * 16, 0, AUX_SC1);
      }
      if (it == 7) {
        f32x4 s = (__builtin_bit_cast(f32x4, pl[0]) + __builtin_bit_cast(f32x4, pl[1])) +
                  (__builtin_bit_cast(f32x4, pl[2]) + __builtin_bit_cast(f32x4, pl[3]));
#pragma unroll
        for (int e = 0; e < 4; ++e) s[e] += __shfl_xor(s[e], 32);
        if (lane < 32) *(LAS f32x4*)(size_t)(LB + RED + w * 512 + lane * 16) = s;
      }
      if (it == 8 && w == 0 && lane < 32) {
        const unsigned ra = LB + RED + lane * 16;
        const f32x4 s = (*(LAS f32x4*)(size_t)ra + *(LAS f32x4*)(size_t)(ra + 512)) + (*(LAS f32x4*)(size_t)(ra + 1024) + *(LAS f32x4*)(size_t)(ra + 1536));
        __builtin_amdgcn_raw_buffer_store_b128(__builtin_bit_cast(u32x4, s), wsr, WS_S + g * 16384 + t * 512 + lane * 16, 0, AUX_SC1);
      }
      kstep9<64>(acc, A, B0, B1, a1, a0, bc);
      kstep9<128>(acc, A, B1, B0, a0, a1, bc);
      bc += 128;
    }
    {
    const int lane = fresh_lane(), g4 = lane >> 4;
#pragma unroll
    for (int blk = 0; blk < 9; ++blk)
#pragma unroll
      for (int tt = 0; tt < 4; ++tt) {
        const f32x4 v = acc[blk][tt];
        u32x2 o;
        o[0] = pk2(fmaxf(v[0], 0.f) * v[0], fmaxf(v[1], 0.f) * v[1]);
        o[1] = pk2(fmaxf(v[2], 0.f) * v[2], fmaxf(v[3], 0.f) * v[3]);
        *(LAS u32x2*)(size_t)(LB + (16 * tt + (lane & 15)) * ACT_STRIDE + (64 * blk + 16 * w + 4 * g4) * 2) = o;
        acc[blk][tt] = (f32x4){0.f, 0.f, 0.f, 0.f};
      }
    if (w == 0) {
      WAIT_VM(0);
      if (lane == 0) st_flag((unsigned*)(ws + WS_FLAG2) + g * 32 + t, tag);
    }
    }
    WAIT_LGKM(0);
    BAR();
#pragma unroll
    for (int j = 0; j < 4; ++j) B0[j] = lds_b128(b0 + j * 16 * ACT_STRIDE);
    bc = b0;
    u32x4 sl[4];
    kstep9<64>(acc, A, B0, B1, a1, a0, bc);
#pragma unroll 1
    for (int it = 0; it < 10; ++it) {
      const int lane = fresh_lane(), g4 = lane >> 4;
      if (it == 3) fl = lane < 32 ? ld_flag((const unsigned*)(ws + WS_FLAG2) + g * 32 + lane) : tag;
      if (it == 4) {
        wait_flags((const unsigned*)(ws + WS_FLAG2) + g * 32, 32, tag, lane, fl);
#pragma unroll
        for (int n = 0; n < 4; ++n)
          sl[n] = __builtin_amdgcn_raw_buffer_load_b128(wsr, WS_S + g * 16384 + (4 * w + n) * 1024 + lane * 16, 0, AUX_SC1);
      }
      if (it == 5) {
        const unsigned ti = (unsigned)(0x4433322221111000ull >> (16 * w)) & 0xffffu;
        const unsigned tj = (unsigned)(0x4343243214210410ull >> (16 * w)) & 0xffffu;
#pragma unroll
        for (int n = 0; n < 4; ++n) {
          const int d0 = 16 * (int)((ti >> (4 * n)) & 15u) + 4 * g4, e = 16 * (int)((tj >> (4 * n)) & 15u) + (lane & 15);
          const f32x4 v = __builtin_bit_cast(f32x4, sl[n]);
#pragma unroll
          for (int hh = 0; hh < 2; ++hh) {
            const int d = d0 + 2 * hh;
            const int hd = d / 18, he = e / 18;
            const bool ok = d < 72 && (e == 72 || (e < 72 && hd == he));
            const int row = e == 72 ? 18 : e - 18 * he;
            if (ok) *(LAS unsigned*)(size_t)(LB + SIMG + hd * 684 + row * 36 + (d - 18 * hd) * 2) = pk2(v[2 * hh], v[2 * hh + 1]);
          }
        }
      }
      if (it == 8) bc -= 18 * 64;
      kstep9<128>(acc, A, B1, B0, a0, a1, bc);
      kstep9<192>(acc, A, B0, B1, a1, a0, bc);
      bc += 128;
    }
    {
      const int lane = fresh_lane(), g4 = lane >> 4;
      const int hf = (lane >> 3) & 1, l32 = g4 * 8 + (lane & 7);
#pragma unroll
      for (int tt = 0; tt < 4; ++tt) {
        const unsigned sb = WS_SLAB + ((t * 8 + 2 * tt + hf) * 8 + g) * 9216;
#pragma unroll
        for (int j = 0; j < 4; ++j) {
          u32x4 o;
          o[0] = pk2(acc[2 * j][tt][0], acc[2 * j][tt][1]); o[1] = pk2(acc[2 * j][tt][2], acc[2 * j][tt][3]);
          o[2] = pk2(acc[2 * j + 1][tt][0], acc[2 * j + 1][tt][1]); o[3] = pk2(acc[2 * j + 1][tt][2], acc[2 * j + 1][tt][3]);
          __builtin_amdgcn_raw_buffer_store_b128(o, wsr, sb + ((w * 4 + j) * 32 + l32) * 16, 0, AUX_SC1);
        }
        u32x2 o2;
        o2[0] = pk2(acc[8][tt][0], acc[8][tt][1]); o2[1] = pk2(acc[8][tt][2], acc[8][tt][3]);
        __builtin_amdgcn_raw_buffer_store_b64(o2, wsr, sb + 8192 + (w * 32 + l32) * 8, 0, AUX_SC1);
      }
    }
    WAIT_VM(0);
  }
  BAR();
  {
  int tid = threadIdx.x; asm volatile("" : "+v"(tid));
  const int lane = tid & 63;
  if (tid == 0) st_flag((unsigned*)(ws + WS_FLAG3) + t * 32 + g, tag);
  const int c0 = tid, c1 = tid + 512;
  const unsigned short* xrow = x + (size_t)(t * 64 + g * 8) * DM;
  u32x4 xa = *(const u32x4*)(xrow + (size_t)c0 * 8), ba = *(const u32x4*)(b2 + (c0 % 72) * 8);
  u32x4 xb = xa, bb = ba;
  if (tid < 64) { xb = *(const u32x4*)(xrow + (size_t)c1 * 8); bb = *(const u32x4*)(b2 + (c1 % 72) * 8); }
  {
    const unsigned f0 = lane < 8 ? ld_flag((const unsigned*)(ws + WS_FLAG3) + t * 32 + lane) : tag;
    wait_flags((const unsigned*)(ws + WS_FLAG3) + t * 32, 8, tag, lane, f0);
  }
  {
    const unsigned sb = WS_SLAB + (t * 8 + g) * 8 * 9216;
    u32x4 v[8];
    u32x2 r[8];
#pragma unroll
    for (int s = 0; s < 8; ++s) v[s] = __builtin_amdgcn_raw_buffer_load_b128(wsr, sb + s * 9216 + tid * 16, 0, AUX_SC1);
    if (tid < 128) {
#pragma unroll
      for (int s = 0; s < 8; ++s) r[s] = __builtin_amdgcn_raw_buffer_load_b64(wsr, sb + s * 9216 + 8192 + tid * 8, 0, AUX_SC1);
    }
    f32x4 lo = {0.f, 0.f, 0.f, 0.f}, hi = {0.f, 0.f, 0.f, 0.f};
#pragma unroll
    for (int s = 0; s < 8; ++s) {
      lo += (f32x4){bf_lo(v[s][0]), bf_hi(v[s][0]), bf_lo(v[s][1]), bf_hi(v[s][1])};
      hi += (f32x4){bf_lo(v[s][2]), bf_hi(v[s][2]), bf_lo(v[s][3]), bf_hi(v[s][3])};
    }
    const int cw = tid >> 7, cj = (tid >> 5) & 3, cg = (tid >> 3) & 3, tk = tid & 7;
    const unsigned fa = LB + tk * 2320 + (128 * cj + 16 * cw + 4 * cg) * 4;
    *(LAS f32x4*)(size_t)fa = lo;
    *(LAS f32x4*)(size_t)(fa + 256) = hi;
    if (tid < 128) {
      f32x4 m = {0.f, 0.f, 0.f, 0.f};
#pragma unroll
      for (int s = 0; s < 8; ++s) m += (f32x4){bf_lo(r[s][0]), bf_hi(r[s][0]), bf_lo(r[s][1]), bf_hi(r[s][1])};
      const int rw = tid >> 5, rg = (tid >> 3) & 3;
      *(LAS f32x4*)(size_t)(LB + tk * 2320 + (512 + 16 * rw + 4 * rg) * 4) = m;
    }
  }
  __syncthreads();
  {
    auto fin = [&](int c, u32x4 xv, u32x4 bv) {
      const unsigned fa = LB + (c / 72) * 2320 + (c % 72) * 32;
      const f32x4 lo = *(LAS f32x4*)(size_t)fa, hi = *(LAS f32x4*)(size_t)(fa + 16);
      u32x4 o;
      o[0] = pk2(lo[0] + bf_lo(xv[0]) + bf_lo(bv[0]), lo[1] + bf_hi(xv[0]) + bf_hi(bv[0]));
      o[1] = pk2(lo[2] + bf_lo(xv[1]) + bf_lo(bv[1]), lo[3] + bf_hi(xv[1]) + bf_hi(bv[1]));
      o[2] = pk2(hi[0] + bf_lo(xv[2]) + bf_lo(bv[2]), hi[1] + bf_hi(xv[2]) + bf_hi(bv[2]));
      o[3] = pk2(hi[2] + bf_lo(xv[3]) + bf_lo(bv[3]), hi[3] + bf_hi(xv[3]) + bf_hi(bv[3]));
      *(u32x4*)(out + (size_t)(t * 64 + g * 8) * DM + (size_t)c * 8) = o;
    };
    fin(c0, xa, ba);
    if (tid < 64) fin(c1, xb, bb);
  }
  if (blockIdx.x == 0 && tid == 0) st_flag((unsigned*)(ws + WS_EPOCH), tag);
  }
}

extern "C" void kernel_launch(void* const* d_in, const int* in_sizes, int n_in, void* d_out, int out_size, void* d_ws,
                              size_t ws_size, hipStream_t stream) {
  (void)in_sizes; (void)n_in; (void)out_size; (void)ws_size;
  hipFuncSetAttribute((const void*)fused_block_kernel, hipFuncAttributeMaxDynamicSharedMemorySize, LDS_BYTES);
  fused_block_kernel<<<256, 512, LDS_BYTES, stream>>>(
      (const unsigned short*)d_in[0], (const char*)d_in[1], (const char*)d_in[2], (const char*)d_in[4],
      (const unsigned short*)d_in[5], (const char*)d_in[6], (const unsigned short*)d_in[7], (unsigned short*)d_out, (char*)d_ws);
}
```

```cpp
#include <hip/hip_runtime.h>
#include <hip/hip_bf16.h>
#include <type_traits>

typedef __attribute__((ext_vector_type(4))) short s16x4;
typedef __attribute__((ext_vector_type(8))) __bf16 bf16x8;
typedef __attribute__((ext_vector_type(2))) __bf16 bf16x2;
typedef __attribute__((ext_vector_type(4))) float f32x4;
typedef __attribute__((ext_vector_type(2))) float f32x2;
typedef __attribute__((ext_vector_type(4))) unsigned u32x4;
typedef __attribute__((ext_vector_type(2))) unsigned u32x2;
#define LAS __attribute__((address_space(3)))
#define GAS __attribute__((address_space(1)))
#define DI __device__ __forceinline__
#define SCHED() __builtin_amdgcn_sched_barrier(0)
#define BAR() __builtin_amdgcn_s_barrier()
#define WAIT_VM(n) asm volatile("s_waitcnt vmcnt(%0)" ::"n"(n) : "memory")
#define WAIT_LGKM(n) asm volatile("s_waitcnt lgkmcnt(%0)" ::"n"(n) : "memory")

#define STAMP(i) do {} while (0)
#define STAMP_RT(i) do {} while (0)
#define STAMP_DECL
#define STAMP_FLUSH() do {} while (0)

#define MAP_NGX 2

namespace {

constexpr int DM = 576;
constexpr int ACT_STRIDE = 1184;
constexpr int RING = 64 * ACT_STRIDE;
constexpr int KSLOT = 36864;
constexpr int PQ = RING + 2 * KSLOT;
constexpr int PQ_STRIDE = 144;
constexpr int SIMG = PQ + 64 * PQ_STRIDE;
constexpr int LDS_BYTES = 163840;
constexpr int KTMP = RING + 16384;
constexpr int VTMP = KTMP + 64 * 144;
constexpr int FIN_STRIDE = 2320;
static_assert(SIMG + 5120 <= LDS_BYTES, "lds");
static_assert(VTMP + 64 * 152 + 16 <= RING + KSLOT, "lds");

constexpr unsigned WS_EPOCH = 0;
constexpr unsigned WS_FLAG1 = 1024;
constexpr unsigned WS_FLAG2 = 2048;
constexpr unsigned WS_FLAG3 = 4096;
constexpr unsigned WS_P = 65536;
constexpr unsigned WS_S = WS_P + 256 * 16384;
constexpr unsigned WS_SLAB = WS_S + 8 * 8192;
constexpr unsigned WS_END = WS_SLAB + 32 * 8 * 8 * 9216;
constexpr unsigned OOB = 0x7ffffff0u;
constexpr int AUX_SC1 = 16;
constexpr unsigned SPIN_CAP = 1u << 16;

DI float bf_lo(unsigned u) { return __builtin_bit_cast(float, u << 16); }
DI float bf_hi(unsigned u) { return __builtin_bit_cast(float, u & 0xffff0000u); }
DI unsigned pk2(float a, float b) {
  f32x2 p = {a, b};
  return __builtin_bit_cast(unsigned, __builtin_convertvector(p, bf16x2));
}
DI float rbf(float a) { return bf_lo(pk2(a, a)); }
template <int CTRL> DI float dpp_add(float v) {
  return v + __builtin_bit_cast(float, __builtin_amdgcn_update_dpp(0, __builtin_bit_cast(int, v), CTRL, 0xf, 0xf, true));
}
DI int fresh_lane() { int v = threadIdx.x & 63; asm volatile("" : "+v"(v)); return v; }
DI bf16x8 lds_b128(unsigned addr) { return *(LAS bf16x8*)(size_t)addr; }
DI s16x4 lds_tr(unsigned addr) { return __builtin_amdgcn_ds_read_tr16_b64_v4i16((LAS s16x4*)(size_t)addr); }
DI bf16x8 join(s16x4 lo, s16x4 hi) {
  return __builtin_bit_cast(bf16x8, __builtin_shufflevector(lo, hi, 0, 1, 2, 3, 4, 5, 6, 7));
}
DI f32x4 mfma(bf16x8 a, bf16x8 b, f32x4 c) { return __builtin_amdgcn_mfma_f32_16x16x32_bf16(a, b, c, 0, 0, 0); }
DI float relu2(float v) { return __builtin_amdgcn_fmed3f(v, 0.f, __builtin_inff()) * v; }
DI float phi(float v) {
  float e = __builtin_amdgcn_exp2f(v * 1.44269504088896f);
  return (v > 0.f ? v + 1.f : e) + 1e-6f;
}
DI unsigned ld_flag(const unsigned* p) { return __hip_atomic_load((GAS unsigned*)p, __ATOMIC_RELAXED, __HIP_MEMORY_SCOPE_AGENT); }
DI void st_flag(unsigned* p, unsigned v) { __hip_atomic_store((GAS unsigned*)p, v, __ATOMIC_RELAXED, __HIP_MEMORY_SCOPE_AGENT); }
DI void wait_flags(const unsigned* base, int count, unsigned tag, int lane, unsigned first) {
  unsigned v = first;
#pragma unroll 1
  for (unsigned spins = 0; spins < SPIN_CAP; ++spins) {
    if (__all(lane >= count || v == tag)) break;
    __builtin_amdgcn_s_sleep(2);
    if (lane < count) v = ld_flag(base + lane);
  }
  asm volatile("" ::: "memory");
}
DI f32x4 unpk_lo(u32x4 v) { return (f32x4){bf_lo(v[0]), bf_hi(v[0]), bf_lo(v[1]), bf_hi(v[1])}; }
DI f32x4 unpk_hi(u32x4 v) { return (f32x4){bf_lo(v[2]), bf_hi(v[2]), bf_lo(v[3]), bf_hi(v[3])}; }

DI void dma(unsigned voff, const char* sbase, unsigned m0v) {
  asm volatile("s_mov_b32 m0, %1\n\ts_nop 0\n\tglobal_load_lds_dwordx4 %0, %2" ::"v"(voff), "s"(m0v), "s"(sbase) : "memory");
}
template <int B, int P> DI void piece(const unsigned (&voff)[5], const char* sbase, unsigned m) {
  asm volatile("s_add_i32 m0, %1, %3\n\ts_nop 0\n\tglobal_load_lds_dwordx4 %0, %2 offset:%4"
               ::"v"(voff[P]), "s"(m), "s"(sbase), "n"(B * 4096 + P * 1024 - B * 128), "n"(B * 128) : "memory", "scc");
}
DI void touch(const char* sbase, unsigned voff, unsigned& dummy) { asm volatile("global_load_dword %0, %1, %2" : "+v"(dummy) : "v"(voff), "s"(sbase) : "memory"); }

template <int BOFF0>
DI void kstep9(f32x4 (&acc)[9][4], bf16x8 (&A)[9], const bf16x8 (&Bc)[4], bf16x8 (&Bn)[4], unsigned ab, unsigned abn, unsigned bn) {
  s16x4 lx, ly;
  constexpr int BTS = 16 * ACT_STRIDE;
#define MM(blk, j) acc[blk][j] = mfma(A[blk], Bc[j], acc[blk][j])
#define RL(v, base, blk) v = lds_tr((base) + (blk) * 4096)
#define RH(v, base, blk) A[blk] = join(v, lds_tr((base) + (blk) * 4096 + 512))
  RL(lx, ab, 3); SCHED(); MM(0, 0); SCHED(); RH(lx, ab, 3); SCHED(); MM(0, 1); SCHED(); MM(0, 2); MM(0, 3); SCHED();
  MM(1, 0); SCHED(); Bn[0] = lds_b128(bn + BOFF0); SCHED(); MM(1, 1); SCHED();
  Bn[1] = lds_b128(bn + BOFF0 + BTS); SCHED(); MM(1, 2); MM(1, 3); SCHED();
  WAIT_LGKM(2);
  BAR();
  RL(lx, ab, 4); SCHED(); MM(2, 0); SCHED(); RH(lx, ab, 4); SCHED(); MM(2, 1); SCHED();
  RL(ly, ab, 5); SCHED(); MM(2, 2); SCHED(); RH(ly, ab, 5); SCHED(); MM(2, 3); SCHED();
  RL(lx, ab, 6); SCHED(); MM(3, 0); SCHED(); RH(lx, ab, 6); SCHED(); MM(3, 1); SCHED(); MM(3, 2); MM(3, 3); SCHED();
  RL(lx, ab, 7); SCHED(); MM(4, 0); SCHED(); RH(lx, ab, 7); SCHED(); MM(4, 1); SCHED(); MM(4, 2); MM(4, 3); SCHED();
  RL(lx, ab, 8); SCHED(); MM(5, 0); SCHED(); RH(lx, ab, 8); SCHED(); MM(5, 1); SCHED(); MM(5, 2); MM(5, 3); SCHED();
  MM(6, 0); SCHED(); Bn[2] = lds_b128(bn + BOFF0 + 2 * BTS); SCHED(); MM(6, 1); SCHED();
  Bn[3] = lds_b128(bn + BOFF0 + 3 * BTS); SCHED(); MM(6, 2); MM(6, 3); SCHED();
  WAIT_LGKM(2);
  BAR();
  RL(lx, abn, 0); SCHED(); MM(7, 0); SCHED(); RH(lx, abn, 0); SCHED(); MM(7, 1); SCHED();
  RL(ly, abn, 1); SCHED(); MM(7, 2); SCHED(); RH(ly, abn, 1); SCHED(); MM(7, 3); SCHED();
  RL(lx, abn, 2); SCHED(); MM(8, 0); SCHED(); RH(lx, abn, 2); SCHED(); MM(8, 1); SCHED(); MM(8, 2); MM(8, 3); SCHED();
}
template <int BOFF0>
DI void kstep4(f32x4 (&acc)[4][4], bf16x8 (&A)[4], const bf16x8 (&Bc)[4], bf16x8 (&Bn)[4], unsigned ab, unsigned abn, unsigned d3, unsigned bn) {
  s16x4 lx, ly;
  constexpr int BTS = 16 * ACT_STRIDE;
#define QL(v, base, off) v = lds_tr((base) + (off))
#define QH(v, i, base, off) A[i] = join(v, lds_tr((base) + (off) + 576))
  const unsigned a3 = ab + d3;
  QL(lx, a3, 0); SCHED(); MM(0, 0); SCHED(); QH(lx, 3, a3, 0); SCHED(); MM(0, 1); SCHED();
  Bn[0] = lds_b128(bn + BOFF0); SCHED(); MM(0, 2); SCHED(); Bn[1] = lds_b128(bn + BOFF0 + BTS); SCHED(); MM(0, 3); SCHED();
  Bn[2] = lds_b128(bn + BOFF0 + 2 * BTS); SCHED(); MM(1, 0); SCHED(); Bn[3] = lds_b128(bn + BOFF0 + 3 * BTS); SCHED();
  MM(1, 1); MM(1, 2); MM(1, 3); SCHED();
  WAIT_LGKM(4);
  BAR();
  QL(lx, abn, 0); SCHED(); MM(2, 0); SCHED(); QH(lx, 0, abn, 0); SCHED(); MM(2, 1); SCHED();
  QL(ly, abn, 5120); SCHED(); MM(2, 2); SCHED(); QH(ly, 1, abn, 5120); SCHED(); MM(2, 3); SCHED();
  QL(lx, abn, 10240); SCHED(); MM(3, 0); SCHED(); QH(lx, 2, abn, 10240); SCHED(); MM(3, 1); MM(3, 2); MM(3, 3); SCHED();
}
#undef MM

}

__global__ __launch_bounds__(512, 2) void fused_block_kernel(
    const unsigned short* __restrict__ x, const char* __restrict__ wqkv, const char* __restrict__ wo,
    const char* __restrict__ w1, const unsigned short* __restrict__ b1, const char* __restrict__ w2,
    const unsigned short* __restrict__ b2, unsigned short* __restrict__ out, char* __restrict__ ws) {
  extern __shared__ __attribute__((aligned(16))) char lds[];
  const int tid = threadIdx.x, lane = tid & 63;
  const int wid = __builtin_amdgcn_readfirstlane(tid >> 6);
  constexpr int NGX = MAP_NGX, PFN = 32 / NGX;
  const int xj = blockIdx.x & 7, xi = blockIdx.x >> 3;
  const int g = NGX * (xj / NGX) + xi % NGX, pfr = xi / NGX, t = (xj % NGX) * PFN + pfr;
  const unsigned LB = (unsigned)(size_t)lds;
  const __amdgpu_buffer_rsrc_t wsr = __builtin_amdgcn_make_buffer_rsrc(ws, 0, WS_END, 0x00020000);

  STAMP_DECL
  STAMP(0); STAMP_RT(20);
  const int lr = lane >> 3, lc = lane & 7;
  const int l = wid - 4;
  const int cE = lc ^ (((lr >> 1) & 1) << 1), cO = cE ^ 4;
  unsigned voff[5];
  const unsigned m0r = LB + RING;
  const char* baseQ = wqkv + g * 144 + l * 1152;
  auto issueQ = [&](const char* sq, unsigned mq) {
    dma(voff[0], sq, mq); dma(voff[1], sq, mq + 1024); dma(voff[2], sq, mq + 2048); dma(voff[3], sq, mq + 3072); dma(voff[4], sq, mq + 4096);
  };
  unsigned dummy = 0;
  if (wid == 7) {
    constexpr int RW = 576 / PFN, NQ = (RW + 7) / 8;
    const char* pq_ = wqkv + (size_t)(RW * pfr) * 3456 + ((g * 144) & ~127);
    const int c6 = lc < 5 ? lc : 5;
#pragma unroll
    for (int n = 0; n < NQ; ++n) {
      const int r = 8 * n + lr < RW ? 8 * n + lr : RW - 1;
      touch(pq_, r * 3456 + (c6 >> 1) * 1152 + (c6 & 1) * 128, dummy);
    }
  }
  u32x4 xr[9];
  {
    const char* xp = (const char*)x + (size_t)(t * 64 + wid * 8) * 1152;
    const unsigned xo = lr * 1152 + lc * 16;
#pragma unroll
    for (int i = 0; i < 9; ++i)
      asm volatile("global_load_dwordx4 %0, %1, %2 offset:%3 nt" : "=v"(xr[i]) : "v"(xo), "s"(xp), "n"(i * 128) : "memory");
  }
  unsigned epoch;
  asm volatile("global_load_dword %0, %1, %2 sc1" : "=v"(epoch) : "v"(0u), "s"(ws) : "memory");
  if (wid >= 4 && wid < 7) {
#pragma unroll
    for (int p = 0; p < 5; ++p) {
      int i = 64 * p + lane; i = i < 288 ? i : 287;
      const int r = (i * 7282) >> 16;
      voff[p] = r * 3456 + (i - 9 * r) * 16;
    }
  }
  if (wid == 7) {
    WAIT_VM(0);
  } else if (wid >= 4) {
    issueQ(baseQ, m0r + l * 5120);
    WAIT_VM(5);
  } else {
    WAIT_VM(0);
  }
  SCHED();
#pragma unroll
  for (int i = 0; i < 9; ++i) asm volatile("" : "+v"(xr[i]));
  asm volatile("" : "+v"(epoch));
  const unsigned tag = __builtin_amdgcn_readfirstlane(epoch) + 1u;
  STAMP(1);

  {
    const bf16x2 ones = __builtin_bit_cast(bf16x2, 0x3f803f80u);
    float s0 = 0.f, s1 = 0.f, q0 = 0.f, q1 = 0.f;
#pragma unroll
    for (int i = 0; i < 9; ++i) {
      const unsigned u0 = xr[i][0], u1 = xr[i][1], u2 = xr[i][2], u3 = xr[i][3];
      const bf16x2 a = __builtin_bit_cast(bf16x2, u0), b = __builtin_bit_cast(bf16x2, u1);
      const bf16x2 c = __builtin_bit_cast(bf16x2, u2), d = __builtin_bit_cast(bf16x2, u3);
      s0 = __builtin_amdgcn_fdot2_f32_bf16(a, ones, s0, false); s1 = __builtin_amdgcn_fdot2_f32_bf16(b, ones, s1, false);
      q0 = __builtin_amdgcn_fdot2_f32_bf16(a, a, q0, false); q1 = __builtin_amdgcn_fdot2_f32_bf16(b, b, q1, false);
      s0 = __builtin_amdgcn_fdot2_f32_bf16(c, ones, s0, false); s1 = __builtin_amdgcn_fdot2_f32_bf16(d, ones, s1, false);
      q0 = __builtin_amdgcn_fdot2_f32_bf16(c, c, q0, false); q1 = __builtin_amdgcn_fdot2_f32_bf16(d, d, q1, false);
    }
    float s = s0 + s1, q = q0 + q1;
    s = dpp_add<0xB1>(s); q = dpp_add<0xB1>(q);
    s = dpp_add<0x4E>(s); q = dpp_add<0x4E>(q);
    s = dpp_add<0x141>(s); q = dpp_add<0x141>(q);
    const float mean = s * (1.f / 576.f);
    const float var = fmaxf(q * (1.f / 576.f) - mean * mean, 0.f);
    const float mb = rbf(mean);
    const float sd = rbf(__builtin_amdgcn_sqrtf(rbf(rbf(var) + 1e-5f)));
    const float rs = __builtin_amdgcn_rcpf(sd), nb = -mb * rs;
    const unsigned wa = LB + (wid * 8 + lr) * ACT_STRIDE + lc * 16;
#pragma unroll
    for (int i = 0; i < 9; ++i) {
      u32x4 o;
#pragma unroll
      for (int e = 0; e < 4; ++e) o[e] = pk2(fmaf(bf_lo(xr[i][e]), rs, nb), fmaf(bf_hi(xr[i][e]), rs, nb));
      *(LAS u32x4*)(size_t)(wa + i * 128) = o;
    }
  }
  STAMP(2);

  if (wid >= 4) {
    __builtin_amdgcn_s_setprio(2);
    const char* base1 = w1 + g * 1152;
    const char* base2 = w2 + (size_t)(g * 576) * 1152;
    const char* baseO = wo + (size_t)(g * 72) * 1152;
    auto setvoff = [&](unsigned ldw, int rowsel) {
#pragma unroll
      for (int p = 0; p < 4; ++p) voff[p] = (rowsel * 8 * p + lr) * ldw + ((p & 1) ? cO : cE) * 16;
    };
    bf16x8 ka0, ka1;
    u32x4 pb[4];
    f32x4 num0[4], num1[4];
    auto o_step1 = [&]() {
      const int lane = fresh_lane(), g4 = lane >> 4, r16 = lane & 15;
      const unsigned sa = LB + SIMG + l * 1280 + g4 * 16;
      ka0 = lds_b128(sa + r16 * 64);
      ka1 = lds_b128(sa + (r16 < 3 ? 16 + r16 : 19) * 64);
      const unsigned pa = LB + PQ + r16 * PQ_STRIDE + l * 36 + g4 * 16;
#pragma unroll
      for (int tt = 0; tt < 4; ++tt)
#pragma unroll
        for (int m = 0; m < 4; ++m) pb[tt][m] = *(LAS unsigned*)(size_t)(pa + tt * 16 * PQ_STRIDE + 4 * m);
    };
    auto o_step2 = [&]() {
#pragma unroll
      for (int tt = 0; tt < 4; ++tt) {
        const bf16x8 b = __builtin_bit_cast(bf16x8, pb[tt]);
        num0[tt] = mfma(ka0, b, (f32x4){0.f, 0.f, 0.f, 0.f});
        num1[tt] = mfma(ka1, b, (f32x4){0.f, 0.f, 0.f, 0.f});
      }
    };
    auto o_step3 = [&](auto HALF) {
      constexpr int T0 = 2 * decltype(HALF)::value;
      const int lane = fresh_lane(), g4 = lane >> 4;
      const unsigned oa = LB + (lane & 15) * ACT_STRIDE + l * 36;
#pragma unroll
      for (int tt = T0; tt < T0 + 2; ++tt) {
        const float d2 = num1[tt][2];
        const unsigned du = __builtin_bit_cast(unsigned, d2);
        const unsigned y = (unsigned)__builtin_amdgcn_permlane32_swap(du, du, false, false)[0];
        const unsigned z = (unsigned)__builtin_amdgcn_permlane16_swap(y, y, false, false)[0];
        const float r = __builtin_amdgcn_rcpf(rbf(__builtin_bit_cast(float, z)) + 1e-6f);
        const unsigned o = oa + tt * 16 * ACT_STRIDE;
        *(LAS unsigned*)(size_t)(o + g4 * 8) = pk2(num0[tt][0] * r, num0[tt][1] * r);
        *(LAS unsigned*)(size_t)(o + g4 * 8 + 4) = pk2(num0[tt][2] * r, num0[tt][3] * r);
        const unsigned last = g4 == 0 ? pk2(num1[tt][0] * r, num1[tt][1] * r) : 0u;
        *(LAS unsigned*)(size_t)(g4 == 0 ? o + 32 : o - l * 36 + 144 + l * 12 + (g4 - 1) * 4) = last;
      }
      WAIT_LGKM(0);
    };
    using I0 = std::integral_constant<int, 0>;
    using I1 = std::integral_constant<int, 1>;
    WAIT_LGKM(0);
    if (l < 3) {
      auto prog = [&](auto LC) {
        constexpr int L = decltype(LC)::value;
        auto issue8 = [&](const char* sb, unsigned m) {
          if constexpr (L == 0) { piece<8, 0>(voff, sb, m); piece<8, 3>(voff, sb, m); }
          if constexpr (L == 1) piece<8, 1>(voff, sb, m);
          if constexpr (L == 2) piece<8, 2>(voff, sb, m);
        };
        auto issueLo = [&](const char* sb, unsigned m) {
          if constexpr (L == 0) { piece<0, 2>(voff, sb, m); piece<1, 1>(voff, sb, m); piece<2, 0>(voff, sb, m); piece<2, 3>(voff, sb, m); piece<3, 2>(voff, sb, m); }
          if constexpr (L == 1) { piece<0, 0>(voff, sb, m); piece<0, 3>(voff, sb, m); piece<1, 2>(voff, sb, m); piece<2, 1>(voff, sb, m); piece<3, 0>(voff, sb, m); piece<3, 3>(voff, sb, m); }
          if constexpr (L == 2) { piece<0, 1>(voff, sb, m); piece<1, 0>(voff, sb, m); piece<1, 3>(voff, sb, m); piece<2, 2>(voff, sb, m); piece<3, 1>(voff, sb, m); }
        };
        auto issueHi = [&](const char* sb, unsigned m) {
          if constexpr (L == 0) { piece<4, 1>(voff, sb, m); piece<5, 0>(voff, sb, m); piece<5, 3>(voff, sb, m); piece<6, 2>(voff, sb, m); piece<7, 1>(voff, sb, m); }
          if constexpr (L == 1) { piece<4, 2>(voff, sb, m); piece<5, 1>(voff, sb, m); piece<6, 0>(voff, sb, m); piece<6, 3>(voff, sb, m); piece<7, 2>(voff, sb, m); }
          if constexpr (L == 2) { piece<4, 0>(voff, sb, m); piece<4, 3>(voff, sb, m); piece<5, 2>(voff, sb, m); piece<6, 1>(voff, sb, m); piece<7, 0>(voff, sb, m); piece<7, 3>(voff, sb, m); }
        };
        constexpr int WX = L == 0 ? 10 : 11;
        const unsigned mA = m0r, mB = m0r + KSLOT;
        {
          const char* sq = baseQ + (size_t)32 * 3456;
          const unsigned mq = m0r + L * 5120;
          issueQ(sq, mq + 16384); issueQ(sq + 32 * 3456, mq + KSLOT);
          sq += 64 * 3456;
          WAIT_VM(10);
          BAR();
          STAMP(3);
          issueQ(sq, mq + KSLOT + 16384); WAIT_VM(10); BAR();
          sq += 32 * 3456;
#pragma unroll 1
          for (int j = 0; j < 3; ++j) {
            issueQ(sq, mq); WAIT_VM(10); BAR();
            issueQ(sq + 32 * 3456, mq + 16384); WAIT_VM(10); BAR();
            issueQ(sq + 64 * 3456, mq + KSLOT); WAIT_VM(10); BAR();
            issueQ(sq + 96 * 3456, mq + KSLOT + 16384); WAIT_VM(10); BAR();
            sq += 128 * 3456;
          }
          issueQ(sq, mq); WAIT_VM(10); BAR();
          issueQ(sq + 32 * 3456, mq + 16384); WAIT_VM(10); BAR();
        }
        setvoff(9216, 1);
        const char* sb = base1;
        issueLo(sb, mB); WAIT_VM(10); BAR();
        issueHi(sb, mB); issue8(sb, mB); WAIT_VM(12); BAR();
        issueLo(sb + 32 * 9216, mA); BAR(); BAR();
        WAIT_VM(12); BAR();
        auto kstepL = [&](const char* s1, unsigned m1, const char* s2, unsigned m2, auto mid) {
          issueHi(s1, m1); mid(); WAIT_VM(WX); BAR();
          issue8(s1, m1); issueLo(s2, m2); WAIT_VM(12); BAR();
        };
        auto nop = []() {};
        sb = base1 + 32 * 9216;
#pragma unroll 1
        for (int it = 0; it < 8; ++it) {
          kstepL(sb, mA, sb + 32 * 9216, mB, nop);
          kstepL(sb + 32 * 9216, mB, sb + 64 * 9216, mA, nop);
          sb += 64 * 9216;
        }
        issueHi(sb, mA); WAIT_VM(WX); BAR();
        issue8(sb, mA); setvoff(1152, 1); issueLo(base2, mB); WAIT_VM(12); BAR();
        sb = base2;
        kstepL(sb, mB, sb + 32 * 1152, mA, nop);
        kstepL(sb + 32 * 1152, mA, sb + 64 * 1152, mB, [&]() { BAR(); });
        sb += 64 * 1152;
#pragma unroll 1
        for (int it = 0; it < 5; ++it) {
          kstepL(sb, mB, sb + 32 * 1152, mA, nop);
          kstepL(sb + 32 * 1152, mA, sb + 64 * 1152, mB, nop);
          sb += 64 * 1152;
        }
        kstepL(sb, mB, sb + 32 * 1152, mA, nop);
        kstepL(sb + 32 * 1152, mA, sb + 64 * 1152, mB, o_step1);
        kstepL(sb + 64 * 1152, mB, sb + 96 * 1152, mA, o_step2);
        kstepL(sb + 96 * 1152, mA, sb + 128 * 1152, mB, [&]() { o_step3(I0{}); });
        kstepL(sb + 128 * 1152, mB, sb + 160 * 1152, mA, [&]() { o_step3(I1{}); });
        kstepL(sb + 160 * 1152, mA, baseO, mB, nop);
        kstepL(baseO, mB, baseO + 32 * 1152, mA, nop);
        issueHi(baseO + 32 * 1152, mA); WAIT_VM(WX); BAR();
        issue8(baseO + 32 * 1152, mA); setvoff(1152, 0); issueLo(baseO + 64 * 1152, mB); WAIT_VM(12); BAR();
        issueHi(baseO + 64 * 1152, mB); WAIT_VM(WX); BAR();
        issue8(baseO + 64 * 1152, mB); WAIT_VM(6); BAR();
        WAIT_VM(0); BAR(); BAR();
      };
      if (l == 0) prog(std::integral_constant<int, 0>{});
      else if (l == 1) prog(std::integral_constant<int, 1>{});
      else prog(std::integral_constant<int, 2>{});
    } else {
      auto bars = [&](int n) {
#pragma unroll 1
        for (int i = 0; i < n; ++i) BAR();
      };
      const int lane = fresh_lane();
      constexpr int RW = 576 / PFN;
      BAR();
      STAMP(3);
      {
        const char* p1 = w1 + (size_t)(RW * pfr) * 9216 + g * 1152;
        const int c9 = (lane & 15) < 8 ? (lane & 15) : 8;
        constexpr int N1 = (RW + 3) / 4;
        static_assert(N1 <= 17, "prefetch steps");
#pragma unroll 1
        for (int n = 0; n < N1; ++n) {
          const int r = 4 * n + (lane >> 4) < RW ? 4 * n + (lane >> 4) : RW - 1;
          touch(p1, r * 9216 + c9 * 128, dummy);
          BAR();
        }
        bars(17 - N1 + 2 + 1);
      }
      {
        const char* p2 = base2 + (size_t)(RW * pfr) * 1152;
#pragma unroll
        for (int n = 0; n < (RW * 9 + 63) / 64; ++n) {
          const int i = lane + 64 * n < RW * 9 ? lane + 64 * n : 0;
          touch(p2, i * 128, dummy);
        }
        constexpr int WOL = (648 + PFN - 1) / PFN;
#pragma unroll
        for (int n = 0; n < (WOL + 63) / 64; ++n) {
          int i = pfr * WOL + lane + 64 * n; i = (lane + 64 * n < WOL && i < 648) ? i : 0;
          touch(baseO, i * 128, dummy);
        }
      }
      bars(13);
      if (lane == 0) st_flag((unsigned*)(ws + WS_FLAG1) + g * 32 + t, tag);
      bars(7);
      unsigned fl = lane < 32 ? ld_flag((const unsigned*)(ws + WS_FLAG1) + g * 32 + lane) : tag;
      bars(3);
      STAMP(16);
      wait_flags((const unsigned*)(ws + WS_FLAG1) + g * 32, 32, tag, lane, fl);
      STAMP(17);
      {
        u32x4 hv[16];
        const unsigned po = WS_P + (g * 32 + (lane >> 5)) * 16384 + t * 512 + (lane & 31) * 16;
#pragma unroll
        for (int n = 0; n < 16; ++n)
          hv[n] = __builtin_amdgcn_raw_buffer_load_b128(wsr, po, n * 32768, AUX_SC1);
        SCHED();
        bars(5);
        f32x4 a = __builtin_bit_cast(f32x4, hv[0]), b = __builtin_bit_cast(f32x4, hv[1]);
#pragma unroll
        for (int n = 2; n < 16; n += 2) { a += __builtin_bit_cast(f32x4, hv[n]); b += __builtin_bit_cast(f32x4, hv[n + 1]); }
        a += b;
        SCHED();
        BAR();
        const int hh = lane >> 5, li = 32 * (t & 1) + (lane & 31), tile = t >> 1;
        float s0 = hh ? a[2] : a[0], s1 = hh ? a[3] : a[1], o0 = hh ? a[0] : a[2], o1 = hh ? a[1] : a[3];
        const unsigned x0 = (unsigned)__builtin_amdgcn_permlane32_swap(__builtin_bit_cast(unsigned, o0), __builtin_bit_cast(unsigned, o0), false, false)[hh ? 0 : 1];
        const unsigned x1 = (unsigned)__builtin_amdgcn_permlane32_swap(__builtin_bit_cast(unsigned, o1), __builtin_bit_cast(unsigned, o1), false, false)[hh ? 0 : 1];
        s0 += __builtin_bit_cast(float, x0); s1 += __builtin_bit_cast(float, x1);
        const int di = (int)((0x4433322221111000ull >> (4 * tile)) & 15u), ej = (int)((0x4343243214210410ull >> (4 * tile)) & 15u);
        const int d = 16 * di + 4 * (li >> 4) + 2 * hh, e = 16 * ej + (li & 15);
        const int hd = (d * 57) >> 10, he = (e * 57) >> 10;
        const bool ok = d < 72 && (e == 72 || (e < 72 && hd == he));
        const int row = e == 72 ? 18 : e - 18 * he;
        __builtin_amdgcn_raw_buffer_store_b32(pk2(s0, s1), wsr, ok ? WS_S + g * 8192 + hd * 1280 + row * 64 + (d - 18 * hd) * 2 : OOB, 0, AUX_SC1);
      }
      bars(5);
      WAIT_VM(0);
      if (lane == 0) st_flag((unsigned*)(ws + WS_FLAG2) + g * 32 + t, tag);
      bars(2 + 1 + 7);
      fl = lane < 32 ? ld_flag((const unsigned*)(ws + WS_FLAG2) + g * 32 + lane) : tag;
      bars(3);
      STAMP(18);
      wait_flags((const unsigned*)(ws + WS_FLAG2) + g * 32, 32, tag, lane, fl);
      STAMP(19);
      {
        u32x4 iv[5];
#pragma unroll
        for (int n = 0; n < 5; ++n) iv[n] = __builtin_amdgcn_raw_buffer_load_b128(wsr, WS_S + g * 8192 + lane * 16, n * 1024, AUX_SC1);
        SCHED();
        bars(5);
        const int c = lane & 3;
#pragma unroll
        for (int n = 0; n < 5; ++n) {
          const int row = 16 * n + (lane >> 2);
          const bool live = row != 19 && row != 39 && row != 59 && row != 79 && c < 3;
          u32x4 v = iv[n];
          v[0] = live ? v[0] : 0u;
          v[1] = (live && c < 2) ? v[1] : 0u; v[2] = (live && c < 2) ? v[2] : 0u; v[3] = (live && c < 2) ? v[3] : 0u;
          *(LAS u32x4*)(size_t)(LB + SIMG + n * 1024 + lane * 16) = v;
        }
        WAIT_LGKM(0);
      }
      bars(9);
      o_step1(); bars(2);
      o_step2(); bars(2);
      o_step3(I0{}); bars(2);
      o_step3(I1{}); bars(12);
    }
    STAMP(8);
    WAIT_VM(0);
    asm volatile("" ::"v"(dummy));
  } else {
    const int w = wid;
    const int g4 = lane >> 4, q = (lane & 15) >> 2, p = lane & 3;
    const int cf = (((q >> 1) & 1) << 1) | ((g4 & 1) << 2);
    unsigned a0 = LB + RING + (8 * g4 + q) * 128 + (((2 * w + (p >> 1)) ^ cf) * 16) + (p & 1) * 8;
    unsigned a1 = a0 + KSLOT;
    asm volatile("" : "+v"(a1));
    const unsigned b0 = LB + (lane & 15) * ACT_STRIDE + g4 * 16;
    bf16x8 B0[4], B1[4];
    f32x4 acc[9][4];
    {
      f32x4 qa[4][4];
      bf16x8 A[4];
#pragma unroll
      for (int i = 0; i < 4; ++i)
#pragma unroll
        for (int j = 0; j < 4; ++j) qa[i][j] = (f32x4){0.f, 0.f, 0.f, 0.f};
      WAIT_LGKM(0);
      BAR();
      STAMP(3);
#pragma unroll
      for (int j = 0; j < 4; ++j) B0[j] = lds_b128(b0 + j * 16 * ACT_STRIDE);
      unsigned ac = LB + RING + (8 * g4 + q) * 144 + 32 * w + 8 * p, an = ac + KSLOT, bc = b0;
      const unsigned d3 = (w < 3 ? w : 0) * 5120 + 128 - 32 * w;
      A[0] = join(lds_tr(ac), lds_tr(ac + 576));
      A[1] = join(lds_tr(ac + 5120), lds_tr(ac + 5120 + 576));
      A[2] = join(lds_tr(ac + 10240), lds_tr(ac + 10240 + 576));
#pragma unroll 1
      for (int it = 0; it < 9; ++it) {
        kstep4<64>(qa, A, B0, B1, ac, ac + 16384, d3, bc);
        kstep4<128>(qa, A, B1, B0, ac + 16384, an, d3, bc);
        const unsigned tmp = ac; ac = an; an = tmp;
        bc += 128;
      }
      STAMP(4);
      const int lane = fresh_lane(), g4 = lane >> 4, q = (lane & 15) >> 2, p = lane & 3;
      u32x2 bb[9];
#pragma unroll
      for (int blk = 0; blk < 9; ++blk) bb[blk] = *(const u32x2*)(b1 + g * 576 + 64 * blk + 16 * w + 4 * g4);
#pragma unroll
      for (int tt = 0; tt < 4; ++tt) {
        const int tok = 16 * tt + (lane & 15);
        u32x2 o;
        o[0] = pk2(phi(qa[0][tt][0]), phi(qa[0][tt][1])); o[1] = pk2(phi(qa[0][tt][2]), phi(qa[0][tt][3]));
        *(LAS u32x2*)(size_t)(LB + PQ + tok * PQ_STRIDE + (16 * w + 4 * g4) * 2) = o;
        o[0] = pk2(phi(qa[1][tt][0]), phi(qa[1][tt][1])); o[1] = pk2(phi(qa[1][tt][2]), phi(qa[1][tt][3]));
        *(LAS u32x2*)(size_t)(LB + KTMP + tok * 144 + (16 * w + 4 * g4) * 2) = o;
        o[0] = pk2(qa[2][tt][0], qa[2][tt][1]); o[1] = pk2(qa[2][tt][2], qa[2][tt][3]);
        *(LAS u32x2*)(size_t)(LB + VTMP + tok * 152 + (16 * w + 4 * g4) * 2) = o;
        if (w < 2) {
          o[0] = pk2(phi(qa[3][tt][0]), phi(qa[3][tt][1])); o[1] = pk2(phi(qa[3][tt][2]), phi(qa[3][tt][3]));
          if (g4 < 2) *(LAS u32x2*)(size_t)(LB + (w == 0 ? PQ : KTMP) + tok * 144 + 128 + g4 * 8) = o;
        } else {
          o[0] = pk2(qa[3][tt][0], qa[3][tt][1]); o[1] = pk2(qa[3][tt][2], qa[3][tt][3]);
          if (w == 3) { o[0] = 0x00003f80u; o[1] = 0u; }
          if (w == 2 ? g4 < 2 : g4 == 0) *(LAS u32x2*)(size_t)(LB + VTMP + tok * 152 + 128 + (w == 2 ? g4 * 8 : 16)) = o;
        }
      }
      WAIT_LGKM(0);
      BAR();
      const unsigned ti = (unsigned)(0x4433322221111000ull >> (16 * w)) & 0xffffu;
      const unsigned tj = (unsigned)(0x4343243214210410ull >> (16 * w)) & 0xffffu;
      const unsigned kr = LB + KTMP + (8 * g4 + q) * 144 + p * 8, vr = LB + VTMP + (8 * g4 + q) * 152 + p * 8;
#pragma unroll
      for (int n = 0; n < 4; ++n) {
        const unsigned di = (ti >> (4 * n)) & 15u, ej = (tj >> (4 * n)) & 15u;
        f32x4 d = {0.f, 0.f, 0.f, 0.f};
#pragma unroll
        for (int ks = 0; ks < 2; ++ks) {
          const unsigned ka = kr + ks * 32 * 144 + di * 32, va = vr + ks * 32 * 152 + ej * 32;
          const bf16x8 fa = join(lds_tr(ka), lds_tr(ka + 4 * 144));
          const bf16x8 fb = join(lds_tr(va), lds_tr(va + 4 * 152));
          d = mfma(fa, fb, d);
        }
        __builtin_amdgcn_raw_buffer_store_b128(__builtin_bit_cast(u32x4, d), wsr,
                                               WS_P + (g * 32 + t) * 16384 + (4 * w + n) * 1024 + lane * 16, 0, AUX_SC1);
      }
#pragma unroll
      for (int i = 0; i < 9; ++i) {
        const f32x4 bv = {bf_lo(bb[i][0]), bf_hi(bb[i][0]), bf_lo(bb[i][1]), bf_hi(bb[i][1])};
#pragma unroll
        for (int j = 0; j < 4; ++j) acc[i][j] = bv;
      }
      WAIT_LGKM(0);
      BAR();
      STAMP(5);
    }
    bf16x8 A[9];
    {
#pragma unroll
      for (int j = 0; j < 4; ++j) B0[j] = lds_b128(b0 + j * 16 * ACT_STRIDE);
      A[0] = join(lds_tr(a1), lds_tr(a1 + 512));
      A[1] = join(lds_tr(a1 + 4096), lds_tr(a1 + 4096 + 512));
      A[2] = join(lds_tr(a1 + 8192), lds_tr(a1 + 8192 + 512));
      unsigned bc = b0;
#pragma unroll 1
      for (int it = 0; it < 9; ++it) {
        if (it == 3) WAIT_VM(0);
        kstep9<64>(acc, A, B0, B1, a1, a0, bc);
        kstep9<128>(acc, A, B1, B0, a0, a1, bc);
        bc += 128;
      }
      STAMP(6);
      const int lane = fresh_lane(), g4 = lane >> 4;
      const unsigned ha = LB + (lane & 15) * ACT_STRIDE + (16 * w + 8 * (g4 >> 1)) * 2;
#pragma unroll
      for (int tt = 0; tt < 4; ++tt) {
#pragma unroll
        for (int j = 0; j < 4; ++j) {
          const f32x4 u = acc[2 * j][tt], v = acc[2 * j + 1][tt];
          const auto s0 = __builtin_amdgcn_permlane16_swap(pk2(relu2(u[0]), relu2(u[1])), pk2(relu2(v[0]), relu2(v[1])), false, false);
          const auto s1 = __builtin_amdgcn_permlane16_swap(pk2(relu2(u[2]), relu2(u[3])), pk2(relu2(v[2]), relu2(v[3])), false, false);
          const u32x4 o = {(unsigned)s0[0], (unsigned)s1[0], (unsigned)s0[1], (unsigned)s1[1]};
          *(LAS u32x4*)(size_t)(ha + tt * 16 * ACT_STRIDE + (128 * j + 64 * (g4 & 1)) * 2) = o;
        }
      }
#pragma unroll
      for (int tp = 0; tp < 2; ++tp) {
        const f32x4 u = acc[8][2 * tp], v = acc[8][2 * tp + 1];
        const auto s0 = __builtin_amdgcn_permlane16_swap(pk2(relu2(u[0]), relu2(u[1])), pk2(relu2(v[0]), relu2(v[1])), false, false);
        const auto s1 = __builtin_amdgcn_permlane16_swap(pk2(relu2(u[2]), relu2(u[3])), pk2(relu2(v[2]), relu2(v[3])), false, false);
        const u32x4 o = {(unsigned)s0[0], (unsigned)s1[0], (unsigned)s0[1], (unsigned)s1[1]};
        *(LAS u32x4*)(size_t)(ha + (2 * tp + (g4 & 1)) * 16 * ACT_STRIDE + 512 * 2) = o;
      }
      WAIT_LGKM(0);
      BAR();
      STAMP(7);
    }
    {
#pragma unroll
      for (int j = 0; j < 4; ++j) B0[j] = lds_b128(b0 + j * 16 * ACT_STRIDE);
#pragma unroll
      for (int i = 0; i < 9; ++i)
#pragma unroll
        for (int j = 0; j < 4; ++j) acc[i][j] = (f32x4){0.f, 0.f, 0.f, 0.f};
      unsigned bc = b0;
      kstep9<64>(acc, A, B0, B1, a1, a0, bc);
#pragma unroll 1
      for (int it = 0; it < 10; ++it) {
        if (it == 8) bc -= 18 * 64;
        kstep9<128>(acc, A, B1, B0, a0, a1, bc);
        kstep9<192>(acc, A, B0, B1, a1, a0, bc);
        bc += 128;
      }
    }
    STAMP(8);
    {
      const int lane = fresh_lane(), g4 = lane >> 4;
      const int hf = (lane >> 3) & 1, l32 = g4 * 8 + (lane & 7);
#pragma unroll
      for (int tt = 0; tt < 4; ++tt) {
        const bool own = 2 * tt + hf == g;
        const unsigned sb = own ? OOB : WS_SLAB + ((t * 8 + 2 * tt + hf) * 8 + g) * 9216;
#pragma unroll
        for (int j = 0; j < 4; ++j) {
          u32x4 o;
          o[0] = pk2(acc[2 * j][tt][0], acc[2 * j][tt][1]); o[1] = pk2(acc[2 * j][tt][2], acc[2 * j][tt][3]);
          o[2] = pk2(acc[2 * j + 1][tt][0], acc[2 * j + 1][tt][1]); o[3] = pk2(acc[2 * j + 1][tt][2], acc[2 * j + 1][tt][3]);
          __builtin_amdgcn_raw_buffer_store_b128(o, wsr, sb + ((w * 4 + j) * 32 + l32) * 16, 0, AUX_SC1);
        }
        u32x2 o2;
        o2[0] = pk2(acc[8][tt][0], acc[8][tt][1]); o2[1] = pk2(acc[8][tt][2], acc[8][tt][3]);
        __builtin_amdgcn_raw_buffer_store_b64(o2, wsr, sb + 8192 + (w * 32 + l32) * 8, 0, AUX_SC1);
        if (tt == (g >> 1) && own) {
          const unsigned fa = LB + (lane & 7) * FIN_STRIDE + (16 * w + 4 * g4) * 4;
#pragma unroll
          for (int blk = 0; blk < 9; ++blk) *(LAS f32x4*)(size_t)(fa + blk * 256) = acc[blk][tt];
        }
      }
    }
    STAMP(9);
    WAIT_VM(0);
    STAMP(10);
  }
  {
    int tid = threadIdx.x; asm volatile("" : "+v"(tid));
    const int c0 = tid, c1 = tid + 512;
    const unsigned short* xrow = x + (size_t)(t * 64 + g * 8) * DM;
    u32x4 xa = *(const u32x4*)(xrow + (size_t)c0 * 8), ba = *(const u32x4*)(b2 + (c0 % 72) * 8);
    u32x4 xb = xa, bb = ba;
    if (tid < 64) { xb = *(const u32x4*)(xrow + (size_t)c1 * 8); bb = *(const u32x4*)(b2 + (c1 % 72) * 8); }
    WAIT_LGKM(0);
    BAR();
    if (tid == 0) st_flag((unsigned*)(ws + WS_FLAG3) + t * 32 + g, tag);
    const unsigned sb = WS_SLAB + (t * 8 + g) * 8 * 9216;
    f32x4 lo = {0.f, 0.f, 0.f, 0.f}, hi = lo, m = lo;
    {
      const int lane = tid & 63;
      const unsigned* f3 = (const unsigned*)(ws + WS_FLAG3) + t * 32;
      const bool poll = lane < 8 && lane != g;
      unsigned f0 = poll ? ld_flag(f3 + lane) : tag;
#pragma unroll 1
      for (unsigned spins = 0; spins < SPIN_CAP; ++spins) {
        if (__all(f0 == tag)) break;
        __builtin_amdgcn_s_sleep(1);
        if (poll) f0 = ld_flag(f3 + lane);
      }
      asm volatile("" ::: "memory");
      STAMP(11);
      u32x4 v[7];
      u32x2 r[7];
#pragma unroll
      for (int s = 0; s < 7; ++s) {
        const unsigned so = sb + ((g + 1 + s) & 7) * 9216;
        v[s] = __builtin_amdgcn_raw_buffer_load_b128(wsr, so + tid * 16, 0, AUX_SC1);
        r[s] = __builtin_amdgcn_raw_buffer_load_b64(wsr, tid < 128 ? so + 8192 + tid * 8 : OOB, 0, AUX_SC1);
      }
#pragma unroll
      for (int s = 0; s < 7; ++s) {
        lo += unpk_lo(v[s]); hi += unpk_hi(v[s]);
        m += (f32x4){bf_lo(r[s][0]), bf_hi(r[s][0]), bf_lo(r[s][1]), bf_hi(r[s][1])};
      }
    }
    STAMP(12);
    const int cw = tid >> 7, cj = (tid >> 5) & 3, cg = (tid >> 3) & 3, tk = tid & 7;
    const unsigned fa = LB + tk * FIN_STRIDE + (128 * cj + 16 * cw + 4 * cg) * 4;
    *(LAS f32x4*)(size_t)fa += lo;
    *(LAS f32x4*)(size_t)(fa + 256) += hi;
    if (tid < 128) *(LAS f32x4*)(size_t)(LB + tk * FIN_STRIDE + (512 + 16 * (tid >> 5) + 4 * cg) * 4) += m;
    STAMP(13);
    __syncthreads();
    auto fin = [&](int c, u32x4 xv, u32x4 bv) {
      const unsigned fb = LB + (c / 72) * FIN_STRIDE + (c % 72) * 32;
      const f32x4 a = *(LAS f32x4*)(size_t)fb + unpk_lo(xv) + unpk_lo(bv), b = *(LAS f32x4*)(size_t)(fb + 16) + unpk_hi(xv) + unpk_hi(bv);
      const u32x4 o = {pk2(a[0], a[1]), pk2(a[2], a[3]), pk2(b[0], b[1]), pk2(b[2], b[3])};
      *(u32x4*)(out + (size_t)(t * 64 + g * 8) * DM + (size_t)c * 8) = o;
    };
    fin(c0, xa, ba);
    if (tid < 64) fin(c1, xb, bb);
    STAMP(14); STAMP_RT(21);
    STAMP_FLUSH();
    if (blockIdx.x == 0 && tid == 0) st_flag((unsigned*)(ws + WS_EPOCH), tag);
  }
}

extern "C" void kernel_launch(void* const* d_in, const int* in_sizes, int n_in, void* d_out, int out_size, void* d_ws,
                              size_t ws_size, hipStream_t stream) {
  (void)in_sizes; (void)n_in; (void)out_size; (void)ws_size;
  (void)hipFuncSetAttribute((const void*)fused_block_kernel, hipFuncAttributeMaxDynamicSharedMemorySize, LDS_BYTES);
  fused_block_kernel<<<256, 512, LDS_BYTES, stream>>>(
      (const unsigned short*)d_in[0], (const char*)d_in[1], (const char*)d_in[2], (const char*)d_in[4],
      (const unsigned short*)d_in[5], (const char*)d_in[6], (const unsigned short*)d_in[7], (unsigned short*)d_out, (char*)d_ws);
}
```

```cpp
#include <hip/hip_runtime.h>
#include <hip/hip_bf16.h>
#include <type_traits>

typedef __attribute__((ext_vector_type(4))) short s16x4;
typedef __attribute__((ext_vector_type(8))) __bf16 bf16x8;
typedef __attribute__((ext_vector_type(2))) __bf16 bf16x2;
typedef __attribute__((ext_vector_type(4))) float f32x4;
typedef __attribute__((ext_vector_type(2))) float f32x2;
typedef __attribute__((ext_vector_type(4))) unsigned u32x4;
typedef __attribute__((ext_vector_type(2))) unsigned u32x2;
#define LAS __attribute__((address_space(3)))
#define GAS __attribute__((address_space(1)))
#define DI __device__ __forceinline__
#define SCHED() __builtin_amdgcn_sched_barrier(0)
#define BAR() __builtin_amdgcn_s_barrier()
#define WAIT_VM(n) asm volatile("s_waitcnt vmcnt(%0)" ::"n"(n) : "memory")
#define WAIT_LGKM(n) asm volatile("s_waitcnt lgkmcnt(%0)" ::"n"(n) : "memory")

#define STAMP(i) do {} while (0)
#define STAMP_RT(i) do {} while (0)
#define STAMP_DECL
#define STAMP_FLUSH() do {} while (0)

#define MAP_NGX 2

namespace {

constexpr int DM = 576;
constexpr int ACT_STRIDE = 1184;
constexpr int RING = 64 * ACT_STRIDE;
constexpr int KSLOT = 36864;
constexpr int PQ = RING + 2 * KSLOT;
constexpr int PQ_STRIDE = 144;
constexpr int SIMG = PQ + 64 * PQ_STRIDE;
constexpr int LDS_BYTES = 163840;
constexpr int KTMP = RING + 16384;
constexpr int VTMP = KTMP + 64 * 144;
constexpr int FIN_STRIDE = 2320;
static_assert(SIMG + 5120 <= LDS_BYTES, "lds");
static_assert(VTMP + 64 * 152 + 16 <= RING + KSLOT, "lds");

constexpr unsigned WS_EPOCH = 0;
constexpr unsigned WS_FLAG1 = 1024;
constexpr unsigned WS_FLAG2 = 2048;
constexpr unsigned WS_FLAG3 = 4096;
constexpr unsigned WS_P = 65536;
constexpr unsigned WS_S = WS_P + 256 * 16384;
constexpr unsigned WS_SLAB = WS_S + 8 * 8192;
constexpr unsigned WS_END = WS_SLAB + 32 * 8 * 8 * 9216;
constexpr unsigned OOB = 0x7ffffff0u;
constexpr int AUX_SC1 = 16;
constexpr unsigned SPIN_CAP = 1u << 16;

DI float bf_lo(unsigned u) { return __builtin_bit_cast(float, u << 16); }
DI float bf_hi(unsigned u) { return __builtin_bit_cast(float, u & 0xffff0000u); }
DI unsigned pk2(float a, float b) {
  f32x2 p = {a, b};
  return __builtin_bit_cast(unsigned, __builtin_convertvector(p, bf16x2));
}
DI float rbf(float a) { return bf_lo(pk2(a, a)); }
template <int CTRL> DI float dpp_add(float v) {
  return v + __builtin_bit_cast(float, __builtin_amdgcn_update_dpp(0, __builtin_bit_cast(int, v), CTRL, 0xf, 0xf, true));
}
DI int fresh_lane() { int v = threadIdx.x & 63; asm volatile("" : "+v"(v)); return v; }
DI bf16x8 lds_b128(unsigned addr) { return *(LAS bf16x8*)(size_t)addr; }
DI s16x4 lds_tr(unsigned addr) { return __builtin_amdgcn_ds_read_tr16_b64_v4i16((LAS s16x4*)(size_t)addr); }
DI bf16x8 join(s16x4 lo, s16x4 hi) {
  return __builtin_bit_cast(bf16x8, __builtin_shufflevector(lo, hi, 0, 1, 2, 3, 4, 5, 6, 7));
}
DI f32x4 mfma(bf16x8 a, bf16x8 b, f32x4 c) { return __builtin_amdgcn_mfma_f32_16x16x32_bf16(a, b, c, 0, 0, 0); }
DI float pos(float v) { const int b = __builtin_bit_cast(int, v); return __builtin_bit_cast(float, b > 0 ? b : 0); }
DI float neg(float v) { const int b = __builtin_bit_cast(int, v); return __builtin_bit_cast(float, b < 0 ? b : 0); }
DI f32x2 relu2(float a, float b) { const f32x2 p = {pos(a), pos(b)}; return p * p; }
DI unsigned phi2(float a, float b) {
  const f32x2 m = (f32x2){a, b} * 1.44269504088896f;
  const f32x2 e = {__builtin_amdgcn_exp2f(neg(m[0])), __builtin_amdgcn_exp2f(neg(m[1]))};
  const f32x2 r = ((f32x2){pos(a), pos(b)} + 1e-6f) + e;
  return __builtin_bit_cast(unsigned, __builtin_convertvector(r, bf16x2));
}
DI unsigned pk2v(f32x2 p) { return __builtin_bit_cast(unsigned, __builtin_convertvector(p, bf16x2)); }
DI unsigned ld_flag(const unsigned* p) { return __hip_atomic_load((GAS unsigned*)p, __ATOMIC_RELAXED, __HIP_MEMORY_SCOPE_AGENT); }
DI void st_flag(unsigned* p, unsigned v) { __hip_atomic_store((GAS unsigned*)p, v, __ATOMIC_RELAXED, __HIP_MEMORY_SCOPE_AGENT); }
DI void wait_flags(const unsigned* base, int count, unsigned tag, int lane, unsigned first) {
  unsigned v = first;
#pragma unroll 1
  for (unsigned spins = 0; spins < SPIN_CAP; ++spins) {
    if (__all(lane >= count || v == tag)) break;
    __builtin_amdgcn_s_sleep(2);
    if (lane < count) v = ld_flag(base + lane);
  }
  asm volatile("" ::: "memory");
}
DI f32x4 unpk_lo(u32x4 v) { return (f32x4){bf_lo(v[0]), bf_hi(v[0]), bf_lo(v[1]), bf_hi(v[1])}; }
DI f32x4 unpk_hi(u32x4 v) { return (f32x4){bf_lo(v[2]), bf_hi(v[2]), bf_lo(v[3]), bf_hi(v[3])}; }

DI void dma(unsigned voff, const char* sbase, unsigned m0v) {
  asm volatile("s_mov_b32 m0, %1\n\ts_nop 0\n\tglobal_load_lds_dwordx4 %0, %2" ::"v"(voff), "s"(m0v), "s"(sbase) : "memory");
}
template <int B, int P> DI void piece(const unsigned (&voff)[5], const char* sbase, unsigned m) {
  asm volatile("s_add_i32 m0, %1, %3\n\ts_nop 0\n\tglobal_load_lds_dwordx4 %0, %2 offset:%4"
               ::"v"(voff[P]), "s"(m), "s"(sbase), "n"(B * 4096 + P * 1024 - B * 128), "n"(B * 128) : "memory", "scc");
}
DI void touch(const char* sbase, unsigned voff, unsigned& dummy) { asm volatile("global_load_dword %0, %1, %2" : "+v"(dummy) : "v"(voff), "s"(sbase) : "memory"); }

template <int BOFF0>
DI void kstep9(f32x4 (&acc)[9][4], bf16x8 (&A)[9], const bf16x8 (&Bc)[4], bf16x8 (&Bn)[4], unsigned ab, unsigned abn, unsigned bn) {
  s16x4 lx, ly;
  constexpr int BTS = 16 * ACT_STRIDE;
#define MM(blk, j) acc[blk][j] = mfma(A[blk], Bc[j], acc[blk][j])
#define RL(v, base, blk) v = lds_tr((base) + (blk) * 4096)
#define RH(v, base, blk) A[blk] = join(v, lds_tr((base) + (blk) * 4096 + 512))
  RL(lx, ab, 3); SCHED(); MM(0, 0); SCHED(); RH(lx, ab, 3); SCHED(); MM(0, 1); SCHED(); MM(0, 2); MM(0, 3); SCHED();
  MM(1, 0); SCHED(); Bn[0] = lds_b128(bn + BOFF0); SCHED(); MM(1, 1); SCHED();
  Bn[1] = lds_b128(bn + BOFF0 + BTS); SCHED(); MM(1, 2); MM(1, 3); SCHED();
  WAIT_LGKM(2);
  BAR();
  RL(lx, ab, 4); SCHED(); MM(2, 0); SCHED(); RH(lx, ab, 4); SCHED(); MM(2, 1); SCHED();
  RL(ly, ab, 5); SCHED(); MM(2, 2); SCHED(); RH(ly, ab, 5); SCHED(); MM(2, 3); SCHED();
  RL(lx, ab, 6); SCHED(); MM(3, 0); SCHED(); RH(lx, ab, 6); SCHED(); MM(3, 1); SCHED(); MM(3, 2); MM(3, 3); SCHED();
  RL(lx, ab, 7); SCHED(); MM(4, 0); SCHED(); RH(lx, ab, 7); SCHED(); MM(4, 1); SCHED(); MM(4, 2); MM(4, 3); SCHED();
  RL(lx, ab, 8); SCHED(); MM(5, 0); SCHED(); RH(lx, ab, 8); SCHED(); MM(5, 1); SCHED(); MM(5, 2); MM(5, 3); SCHED();
  MM(6, 0); SCHED(); Bn[2] = lds_b128(bn + BOFF0 + 2 * BTS); SCHED(); MM(6, 1); SCHED();
  Bn[3] = lds_b128(bn + BOFF0 + 3 * BTS); SCHED(); MM(6, 2); MM(6, 3); SCHED();
  WAIT_LGKM(2);
  BAR();
  RL(lx, abn, 0); SCHED(); MM(7, 0); SCHED(); RH(lx, abn, 0); SCHED(); MM(7, 1); SCHED();
  RL(ly, abn, 1); SCHED(); MM(7, 2); SCHED(); RH(ly, abn, 1); SCHED(); MM(7, 3); SCHED();
  RL(lx, abn, 2); SCHED(); MM(8, 0); SCHED(); RH(lx, abn, 2); SCHED(); MM(8, 1); SCHED(); MM(8, 2); MM(8, 3); SCHED();
}
template <int BOFF0>
DI void kstep4(f32x4 (&acc)[4][4], bf16x8 (&A)[4], const bf16x8 (&Bc)[4], bf16x8 (&Bn)[4], unsigned ab, unsigned abn, unsigned d3, unsigned bn) {
  s16x4 lx, ly;
  constexpr int BTS = 16 * ACT_STRIDE;
#define QL(v, base, off) v = lds_tr((base) + (off))
#define QH(v, i, base, off) A[i] = join(v, lds_tr((base) + (off) + 576))
  const unsigned a3 = ab + d3;
  QL(lx, a3, 0); SCHED(); MM(0, 0); SCHED(); QH(lx, 3, a3, 0); SCHED(); MM(0, 1); SCHED();
  Bn[0] = lds_b128(bn + BOFF0); SCHED(); MM(0, 2); SCHED(); Bn[1] = lds_b128(bn + BOFF0 + BTS); SCHED(); MM(0, 3); SCHED();
  Bn[2] = lds_b128(bn + BOFF0 + 2 * BTS); SCHED(); MM(1, 0); SCHED(); Bn[3] = lds_b128(bn + BOFF0 + 3 * BTS); SCHED();
  MM(1, 1); MM(1, 2); MM(1, 3); SCHED();
  WAIT_LGKM(4);
  BAR();
  QL(lx, abn, 0); SCHED(); MM(2, 0); SCHED(); QH(lx, 0, abn, 0); SCHED(); MM(2, 1); SCHED();
  QL(ly, abn, 5120); SCHED(); MM(2, 2); SCHED(); QH(ly, 1, abn, 5120); SCHED(); MM(2, 3); SCHED();
  QL(lx, abn, 10240); SCHED(); MM(3, 0); SCHED(); QH(lx, 2, abn, 10240); SCHED(); MM(3, 1); MM(3, 2); MM(3, 3); SCHED();
}
#undef MM

}

__global__ __launch_bounds__(512, 2) void fused_block_kernel(
    const unsigned short* __restrict__ x, const char* __restrict__ wqkv, const char* __restrict__ wo,
    const char* __restrict__ w1, const unsigned short* __restrict__ b1, const char* __restrict__ w2,
    const unsigned short* __restrict__ b2, unsigned short* __restrict__ out, char* __restrict__ ws) {
  extern __shared__ __attribute__((aligned(16))) char lds[];
  const int tid = threadIdx.x, lane = tid & 63;
  const int wid = __builtin_amdgcn_readfirstlane(tid >> 6);
  constexpr int NGX = MAP_NGX, PFN = 32 / NGX;
  const int xj = blockIdx.x & 7, xi = blockIdx.x >> 3;
  const int g = NGX * (xj / NGX) + xi % NGX, pfr = xi / NGX, t = (xj % NGX) * PFN + pfr;
  const unsigned LB = (unsigned)(size_t)lds;
  const __amdgpu_buffer_rsrc_t wsr = __builtin_amdgcn_make_buffer_rsrc(ws, 0, WS_END, 0x00020000);

  STAMP_DECL
  STAMP(0); STAMP_RT(20);
  const int lr = lane >> 3, lc = lane & 7;
  const int l = wid - 4;
  const int cE = lc ^ (((lr >> 1) & 1) << 1), cO = cE ^ 4;
  unsigned voff[5];
  const unsigned m0r = LB + RING;
  const char* baseQ = wqkv + g * 144 + l * 1152;
  auto issueQ = [&](const char* sq, unsigned mq) {
    dma(voff[0], sq, mq); dma(voff[1], sq, mq + 1024); dma(voff[2], sq, mq + 2048); dma(voff[3], sq, mq + 3072); dma(voff[4], sq, mq + 4096);
  };
  unsigned dummy = 0;
  u32x4 xr[9];
  {
    const char* xp = (const char*)x + (size_t)(t * 64 + wid * 8) * 1152;
    const unsigned xo = lr * 1152 + lc * 16;
#pragma unroll
    for (int i = 0; i < 9; ++i)
      asm volatile("global_load_dwordx4 %0, %1, %2 offset:%3 nt" : "=v"(xr[i]) : "v"(xo), "s"(xp), "n"(i * 128) : "memory");
  }
  unsigned epoch;
  asm volatile("global_load_dword %0, %1, %2 sc1" : "=v"(epoch) : "v"(0u), "s"(ws) : "memory");
  if (wid >= 4 && wid < 7) {
#pragma unroll
    for (int p = 0; p < 5; ++p) {
      int i = 64 * p + lane; i = i < 288 ? i : 287;
      const int r = (i * 7282) >> 16;
      voff[p] = r * 3456 + (i - 9 * r) * 16;
    }
  }
  if (wid == 7) {
    constexpr int RW = 576 / PFN, NQ = (RW + 7) / 8;
    const char* pq_ = wqkv + (size_t)(RW * pfr) * 3456 + ((g * 144) & ~127);
    const int c6 = lc < 5 ? lc : 5;
#pragma unroll
    for (int n = 0; n < NQ; ++n) {
      const int r = 8 * n + lr < RW ? 8 * n + lr : RW - 1;
      touch(pq_, r * 3456 + (c6 >> 1) * 1152 + (c6 & 1) * 128, dummy);
    }
    WAIT_VM(NQ);
  } else {
    WAIT_VM(0);
  }
  SCHED();
#pragma unroll
  for (int i = 0; i < 9; ++i) asm volatile("" : "+v"(xr[i]));
  asm volatile("" : "+v"(epoch));
  const unsigned tag = __builtin_amdgcn_readfirstlane(epoch) + 1u;
  STAMP(1);

  {
    const bf16x2 ones = __builtin_bit_cast(bf16x2, 0x3f803f80u);
    float s0 = 0.f, s1 = 0.f, q0 = 0.f, q1 = 0.f;
#pragma unroll
    for (int i = 0; i < 9; ++i) {
      const unsigned u0 = xr[i][0], u1 = xr[i][1], u2 = xr[i][2], u3 = xr[i][3];
      const bf16x2 a = __builtin_bit_cast(bf16x2, u0), b = __builtin_bit_cast(bf16x2, u1);
      const bf16x2 c = __builtin_bit_cast(bf16x2, u2), d = __builtin_bit_cast(bf16x2, u3);
      s0 = __builtin_amdgcn_fdot2_f32_bf16(a, ones, s0, false); s1 = __builtin_amdgcn_fdot2_f32_bf16(b, ones, s1, false);
      q0 = __builtin_amdgcn_fdot2_f32_bf16(a, a, q0, false); q1 = __builtin_amdgcn_fdot2_f32_bf16(b, b, q1, false);
      s0 = __builtin_amdgcn_fdot2_f32_bf16(c, ones, s0, false); s1 = __builtin_amdgcn_fdot2_f32_bf16(d, ones, s1, false);
      q0 = __builtin_amdgcn_fdot2_f32_bf16(c, c, q0, false); q1 = __builtin_amdgcn_fdot2_f32_bf16(d, d, q1, false);
    }
    float s = s0 + s1, q = q0 + q1;
    s = dpp_add<0xB1>(s); q = dpp_add<0xB1>(q);
    s = dpp_add<0x4E>(s); q = dpp_add<0x4E>(q);
    s = dpp_add<0x141>(s); q = dpp_add<0x141>(q);
    const float mean = s * (1.f / 576.f);
    const float var = fmaxf(q * (1.f / 576.f) - mean * mean, 0.f);
    const float mb = rbf(mean);
    const float sd = rbf(__builtin_amdgcn_sqrtf(rbf(rbf(var) + 1e-5f)));
    const float rs = __builtin_amdgcn_rcpf(sd), nb = -mb * rs;
    const unsigned wa = LB + (wid * 8 + lr) * ACT_STRIDE + lc * 16;
#pragma unroll
    for (int i = 0; i < 9; ++i) {
      u32x4 o;
#pragma unroll
      for (int e = 0; e < 4; ++e) o[e] = pk2(fmaf(bf_lo(xr[i][e]), rs, nb), fmaf(bf_hi(xr[i][e]), rs, nb));
      *(LAS u32x4*)(size_t)(wa + i * 128) = o;
    }
  }
  STAMP(2);

  if (wid >= 4) {
    __builtin_amdgcn_s_setprio(2);
    const char* base1 = w1 + g * 1152;
    const char* base2 = w2 + (size_t)(g * 576) * 1152;
    const char* baseO = wo + (size_t)(g * 72) * 1152;
    auto setvoff = [&](unsigned ldw, int rowsel) {
#pragma unroll
      for (int p = 0; p < 4; ++p) voff[p] = (rowsel * 8 * p + lr) * ldw + ((p & 1) ? cO : cE) * 16;
    };
    bf16x8 ka0, ka1;
    u32x4 pb[4];
    f32x4 num0[4], num1[4];
    auto o_step1 = [&]() {
      const int lane = fresh_lane(), g4 = lane >> 4, r16 = lane & 15;
      const unsigned sa = LB + SIMG + l * 1280 + g4 * 16;
      ka0 = lds_b128(sa + r16 * 64);
      ka1 = lds_b128(sa + (r16 < 3 ? 16 + r16 : 19) * 64);
      const unsigned pa = LB + PQ + r16 * PQ_STRIDE + l * 36 + g4 * 16;
#pragma unroll
      for (int tt = 0; tt < 4; ++tt)
#pragma unroll
        for (int m = 0; m < 4; ++m) pb[tt][m] = *(LAS unsigned*)(size_t)(pa + tt * 16 * PQ_STRIDE + 4 * m);
    };
    auto o_step2 = [&]() {
#pragma unroll
      for (int tt = 0; tt < 4; ++tt) {
        const bf16x8 b = __builtin_bit_cast(bf16x8, pb[tt]);
        num0[tt] = mfma(ka0, b, (f32x4){0.f, 0.f, 0.f, 0.f});
        num1[tt] = mfma(ka1, b, (f32x4){0.f, 0.f, 0.f, 0.f});
      }
    };
    auto o_step3 = [&](auto HALF) {
      constexpr int T0 = 2 * decltype(HALF)::value;
      const int lane = fresh_lane(), g4 = lane >> 4;
      const unsigned oa = LB + (lane & 15) * ACT_STRIDE + l * 36;
#pragma unroll
      for (int tt = T0; tt < T0 + 2; ++tt) {
        const float d2 = num1[tt][2];
        const unsigned du = __builtin_bit_cast(unsigned, d2);
        const unsigned y = (unsigned)__builtin_amdgcn_permlane32_swap(du, du, false, false)[0];
        const unsigned z = (unsigned)__builtin_amdgcn_permlane16_swap(y, y, false, false)[0];
        const float r = __builtin_amdgcn_rcpf(rbf(__builtin_bit_cast(float, z)) + 1e-6f);
        const unsigned o = oa + tt * 16 * ACT_STRIDE;
        *(LAS unsigned*)(size_t)(o + g4 * 8) = pk2(num0[tt][0] * r, num0[tt][1] * r);
        *(LAS unsigned*)(size_t)(o + g4 * 8 + 4) = pk2(num0[tt][2] * r, num0[tt][3] * r);
        const unsigned last = g4 == 0 ? pk2(num1[tt][0] * r, num1[tt][1] * r) : 0u;
        *(LAS unsigned*)(size_t)(g4 == 0 ? o + 32 : o - l * 36 + 144 + l * 12 + (g4 - 1) * 4) = last;
      }
      WAIT_LGKM(0);
    };
    using I0 = std::integral_constant<int, 0>;
    using I1 = std::integral_constant<int, 1>;
    WAIT_LGKM(0);
    if (l < 3) {
      auto prog = [&](auto LC) {
        constexpr int L = decltype(LC)::value;
        auto issue8 = [&](const char* sb, unsigned m) {
          if constexpr (L == 0) { piece<8, 0>(voff, sb, m); piece<8, 3>(voff, sb, m); }
          if constexpr (L == 1) piece<8, 1>(voff, sb, m);
          if constexpr (L == 2) piece<8, 2>(voff, sb, m);
        };
        auto issueLo = [&](const char* sb, unsigned m) {
          if constexpr (L == 0) { piece<0, 2>(voff, sb, m); piece<1, 1>(voff, sb, m); piece<2, 0>(voff, sb, m); piece<2, 3>(voff, sb, m); piece<3, 2>(voff, sb, m); }
          if constexpr (L == 1) { piece<0, 0>(voff, sb, m); piece<0, 3>(voff, sb, m); piece<1, 2>(voff, sb, m); piece<2, 1>(voff, sb, m); piece<3, 0>(voff, sb, m); piece<3, 3>(voff, sb, m); }
          if constexpr (L == 2) { piece<0, 1>(voff, sb, m); piece<1, 0>(voff, sb, m); piece<1, 3>(voff, sb, m); piece<2, 2>(voff, sb, m); piece<3, 1>(voff, sb, m); }
        };
        auto issueHi = [&](const char* sb, unsigned m) {
          if constexpr (L == 0) { piece<4, 1>(voff, sb, m); piece<5, 0>(voff, sb, m); piece<5, 3>(voff, sb, m); piece<6, 2>(voff, sb, m); piece<7, 1>(voff, sb, m); }
          if constexpr (L == 1) { piece<4, 2>(voff, sb, m); piece<5, 1>(voff, sb, m); piece<6, 0>(voff, sb, m); piece<6, 3>(voff, sb, m); piece<7, 2>(voff, sb, m); }
          if constexpr (L == 2) { piece<4, 0>(voff, sb, m); piece<4, 3>(voff, sb, m); piece<5, 2>(voff, sb, m); piece<6, 1>(voff, sb, m); piece<7, 0>(voff, sb, m); piece<7, 3>(voff, sb, m); }
        };
        constexpr int WX = L == 0 ? 10 : 11;
        const unsigned mA = m0r, mB = m0r + KSLOT;
        {
          const char* sq = baseQ;
          const unsigned mq = m0r + L * 5120;
          issueQ(sq, mq); issueQ(sq + 32 * 3456, mq + 16384); issueQ(sq + 64 * 3456, mq + KSLOT);
          sq += 96 * 3456;
          STAMP(15);
          WAIT_VM(10);
          STAMP(16);
          BAR();
          STAMP(3);
          issueQ(sq, mq + KSLOT + 16384); WAIT_VM(10); BAR();
          sq += 32 * 3456;
#pragma unroll 1
          for (int j = 0; j < 3; ++j) {
            issueQ(sq, mq); WAIT_VM(10); BAR();
            issueQ(sq + 32 * 3456, mq + 16384); WAIT_VM(10); BAR();
            issueQ(sq + 64 * 3456, mq + KSLOT); WAIT_VM(10); BAR();
            issueQ(sq + 96 * 3456, mq + KSLOT + 16384); WAIT_VM(10); BAR();
            sq += 128 * 3456;
          }
          issueQ(sq, mq); WAIT_VM(10); BAR();
          issueQ(sq + 32 * 3456, mq + 16384); WAIT_VM(10); BAR();
        }
        setvoff(9216, 1);
        const char* sb = base1;
        issueLo(sb, mB); WAIT_VM(10); BAR();
        issueHi(sb, mB); issue8(sb, mB); WAIT_VM(12); BAR();
        issueLo(sb + 32 * 9216, mA); BAR(); BAR();
        WAIT_VM(12); BAR();
        auto kstepL = [&](const char* s1, unsigned m1, const char* s2, unsigned m2, auto mid) {
          issueHi(s1, m1); mid(); WAIT_VM(WX); BAR();
          issue8(s1, m1); issueLo(s2, m2); WAIT_VM(12); BAR();
        };
        auto nop = []() {};
        sb = base1 + 32 * 9216;
#pragma unroll 1
        for (int it = 0; it < 8; ++it) {
          kstepL(sb, mA, sb + 32 * 9216, mB, nop);
          kstepL(sb + 32 * 9216, mB, sb + 64 * 9216, mA, nop);
          sb += 64 * 9216;
        }
        issueHi(sb, mA); WAIT_VM(WX); BAR();
        issue8(sb, mA); setvoff(1152, 1); issueLo(base2, mB); WAIT_VM(12); BAR();
        sb = base2;
        kstepL(sb, mB, sb + 32 * 1152, mA, nop);
        kstepL(sb + 32 * 1152, mA, sb + 64 * 1152, mB, [&]() { BAR(); });
        sb += 64 * 1152;
#pragma unroll 1
        for (int it = 0; it < 5; ++it) {
          kstepL(sb, mB, sb + 32 * 1152, mA, nop);
          kstepL(sb + 32 * 1152, mA, sb + 64 * 1152, mB, nop);
          sb += 64 * 1152;
        }
        kstepL(sb, mB, sb + 32 * 1152, mA, nop);
        kstepL(sb + 32 * 1152, mA, sb + 64 * 1152, mB, o_step1);
        kstepL(sb + 64 * 1152, mB, sb + 96 * 1152, mA, o_step2);
        kstepL(sb + 96 * 1152, mA, sb + 128 * 1152, mB, [&]() { o_step3(I0{}); });
        kstepL(sb + 128 * 1152, mB, sb + 160 * 1152, mA, [&]() { o_step3(I1{}); });
        kstepL(sb + 160 * 1152, mA, baseO, mB, nop);
        kstepL(baseO, mB, baseO + 32 * 1152, mA, nop);
        issueHi(baseO + 32 * 1152, mA); WAIT_VM(WX); BAR();
        issue8(baseO + 32 * 1152, mA); setvoff(1152, 0); issueLo(baseO + 64 * 1152, mB); WAIT_VM(12); BAR();
        issueHi(baseO + 64 * 1152, mB); WAIT_VM(WX); BAR();
        issue8(baseO + 64 * 1152, mB); WAIT_VM(6); BAR();
        WAIT_VM(0); BAR(); BAR();
      };
      if (l == 0) prog(std::integral_constant<int, 0>{});
      else if (l == 1) prog(std::integral_constant<int, 1>{});
      else prog(std::integral_constant<int, 2>{});
    } else {
      auto bars = [&](int n) {
#pragma unroll 1
        for (int i = 0; i < n; ++i) BAR();
      };
      const int lane = fresh_lane();
      constexpr int RW = 576 / PFN;
      BAR();
      STAMP(3);
      bars(17 + 1);
      {
        const char* p2 = base2 + (size_t)(RW * pfr) * 1152;
#pragma unroll
        for (int n = 0; n < (RW * 9 + 63) / 64; ++n) {
          const int i = lane + 64 * n < RW * 9 ? lane + 64 * n : 0;
          touch(p2, i * 128, dummy);
        }
        constexpr int WOL = (648 + PFN - 1) / PFN;
#pragma unroll
        for (int n = 0; n < (WOL + 63) / 64; ++n) {
          int i = pfr * WOL + lane + 64 * n; i = (lane + 64 * n < WOL && i < 648) ? i : 0;
          touch(baseO, i * 128, dummy);
        }
      }
      bars(2);
      bars(13);
      if (lane == 0) st_flag((unsigned*)(ws + WS_FLAG1) + g * 32 + t, tag);
      bars(7);
      unsigned fl = lane < 32 ? ld_flag((const unsigned*)(ws + WS_FLAG1) + g * 32 + lane) : tag;
      bars(3);
      STAMP(16);
      wait_flags((const unsigned*)(ws + WS_FLAG1) + g * 32, 32, tag, lane, fl);
      STAMP(17);
      {
        u32x4 hv[16];
        const unsigned po = WS_P + (g * 32 + (lane >> 5)) * 16384 + t * 512 + (lane & 31) * 16;
#pragma unroll
        for (int n = 0; n < 16; ++n)
          hv[n] = __builtin_amdgcn_raw_buffer_load_b128(wsr, po, n * 32768, AUX_SC1);
        SCHED();
        bars(5);
        f32x4 a = __builtin_bit_cast(f32x4, hv[0]), b = __builtin_bit_cast(f32x4, hv[1]);
#pragma unroll
        for (int n = 2; n < 16; n += 2) { a += __builtin_bit_cast(f32x4, hv[n]); b += __builtin_bit_cast(f32x4, hv[n + 1]); }
        a += b;
        SCHED();
        BAR();
        const int hh = lane >> 5, li = 32 * (t & 1) + (lane & 31), tile = t >> 1;
        float s0 = hh ? a[2] : a[0], s1 = hh ? a[3] : a[1], o0 = hh ? a[0] : a[2], o1 = hh ? a[1] : a[3];
        const unsigned x0 = (unsigned)__builtin_amdgcn_permlane32_swap(__builtin_bit_cast(unsigned, o0), __builtin_bit_cast(unsigned, o0), false, false)[hh ? 0 : 1];
        const unsigned x1 = (unsigned)__builtin_amdgcn_permlane32_swap(__builtin_bit_cast(unsigned, o1), __builtin_bit_cast(unsigned, o1), false, false)[hh ? 0 : 1];
        s0 += __builtin_bit_cast(float, x0); s1 += __builtin_bit_cast(float, x1);
        const int di = (int)((0x4433322221111000ull >> (4 * tile)) & 15u), ej = (int)((0x4343243214210410ull >> (4 * tile)) & 15u);
        const int d = 16 * di + 4 * (li >> 4) + 2 * hh, e = 16 * ej + (li & 15);
        const int hd = (d * 57) >> 10, he = (e * 57) >> 10;
        const bool ok = d < 72 && (e == 72 || (e < 72 && hd == he));
        const int row = e == 72 ? 18 : e - 18 * he;
        __builtin_amdgcn_raw_buffer_store_b32(pk2(s0, s1), wsr, ok ? WS_S + g * 8192 + hd * 1280 + row * 64 + (d - 18 * hd) * 2 : OOB, 0, AUX_SC1);
      }
      bars(5);
      WAIT_VM(0);
      if (lane == 0) st_flag((unsigned*)(ws + WS_FLAG2) + g * 32 + t, tag);
      bars(2 + 1 + 7);
      fl = lane < 32 ? ld_flag((const unsigned*)(ws + WS_FLAG2) + g * 32 + lane) : tag;
      bars(3);
      STAMP(18);
      wait_flags((const unsigned*)(ws + WS_FLAG2) + g * 32, 32, tag, lane, fl);
      STAMP(19);
      {
        u32x4 iv[5];
#pragma unroll
        for (int n = 0; n < 5; ++n) iv[n] = __builtin_amdgcn_raw_buffer_load_b128(wsr, WS_S + g * 8192 + lane * 16, n * 1024, AUX_SC1);
        SCHED();
        bars(5);
        const int c = lane & 3;
#pragma unroll
        for (int n = 0; n < 5; ++n) {
          const int row = 16 * n + (lane >> 2);
          const bool live = row != 19 && row != 39 && row != 59 && row != 79 && c < 3;
          u32x4 v = iv[n];
          v[0] = live ? v[0] : 0u;
          v[1] = (live && c < 2) ? v[1] : 0u; v[2] = (live && c < 2) ? v[2] : 0u; v[3] = (live && c < 2) ? v[3] : 0u;
          *(LAS u32x4*)(size_t)(LB + SIMG + n * 1024 + lane * 16) = v;
        }
        WAIT_LGKM(0);
      }
      bars(9);
      o_step1(); bars(2);
      o_step2(); bars(2);
      o_step3(I0{}); bars(2);
      o_step3(I1{}); bars(12);
    }
    STAMP(8);
    WAIT_VM(0);
    asm volatile("" ::"v"(dummy));
  } else {
    const int w = wid;
    const int g4 = lane >> 4, q = (lane & 15) >> 2, p = lane & 3;
    const int cf = (((q >> 1) & 1) << 1) | ((g4 & 1) << 2);
    unsigned a0 = LB + RING + (8 * g4 + q) * 128 + (((2 * w + (p >> 1)) ^ cf) * 16) + (p & 1) * 8;
    unsigned a1 = a0 + KSLOT;
    asm volatile("" : "+v"(a1));
    const unsigned b0 = LB + (lane & 15) * ACT_STRIDE + g4 * 16;
    bf16x8 B0[4], B1[4];
    f32x4 acc[9][4];
    {
      f32x4 qa[4][4];
      bf16x8 A[4];
#pragma unroll
      for (int i = 0; i < 4; ++i)
#pragma unroll
        for (int j = 0; j < 4; ++j) qa[i][j] = (f32x4){0.f, 0.f, 0.f, 0.f};
      {
        constexpr int RW = 576 / PFN, RQ = RW / 4;
        static_assert(RW % 4 == 0 || RW == 18, "rows");
        constexpr int NR = (RW + 3) / 4;
        const int c9 = (lane & 15) < 8 ? (lane & 15) : 8;
        const char* p1 = w1 + (size_t)(RW * pfr) * 9216 + g * 1152;
        (void)RQ;
#pragma unroll
        for (int n = 0; n < (NR + 3) / 4; ++n) {
          int r = w * NR + 4 * n + (lane >> 4);
          r = (4 * n + (lane >> 4) < NR && r < RW) ? r : RW - 1;
          touch(p1, r * 9216 + c9 * 128, dummy);
        }
      }
      STAMP(15);
      WAIT_LGKM(0);
      BAR();
      STAMP(3);
#pragma unroll
      for (int j = 0; j < 4; ++j) B0[j] = lds_b128(b0 + j * 16 * ACT_STRIDE);
      unsigned ac = LB + RING + (8 * g4 + q) * 144 + 32 * w + 8 * p, an = ac + KSLOT, bc = b0;
      const unsigned d3 = (w < 3 ? w : 0) * 5120 + 128 - 32 * w;
      A[0] = join(lds_tr(ac), lds_tr(ac + 576));
      A[1] = join(lds_tr(ac + 5120), lds_tr(ac + 5120 + 576));
      A[2] = join(lds_tr(ac + 10240), lds_tr(ac + 10240 + 576));
#pragma unroll 1
      for (int it = 0; it < 9; ++it) {
        kstep4<64>(qa, A, B0, B1, ac, ac + 16384, d3, bc);
        kstep4<128>(qa, A, B1, B0, ac + 16384, an, d3, bc);
        const unsigned tmp = ac; ac = an; an = tmp;
        bc += 128;
      }
      STAMP(4);
      const int lane = fresh_lane(), g4 = lane >> 4, q = (lane & 15) >> 2, p = lane & 3;
      u32x2 bb[9];
#pragma unroll
      for (int blk = 0; blk < 9; ++blk) bb[blk] = *(const u32x2*)(b1 + g * 576 + 64 * blk + 16 * w + 4 * g4);
      {
        const unsigned tk = (lane & 15), col = (16 * w + 4 * g4) * 2;
        const unsigned aq = LB + PQ + tk * 144 + col, ak = LB + KTMP + tk * 144 + col, av = LB + VTMP + tk * 152 + col;
        constexpr unsigned DROP = 0x40000;
        const unsigned ar = w == 3 ? (g4 == 0 ? LB + VTMP + tk * 152 + 144 : DROP)
                                   : g4 >= 2 ? DROP : (w == 0 ? LB + PQ + tk * 144 : w == 1 ? LB + KTMP + tk * 144 : LB + VTMP + tk * 152) + 128 + g4 * 8;
        const unsigned rs = w >= 2 ? 16 * 152 : 16 * 144;
        u32x2 oq[4], ok[4], ov[4], orm[4];
#pragma unroll
        for (int tt = 0; tt < 4; ++tt) {
          ov[tt] = (u32x2){pk2(qa[2][tt][0], qa[2][tt][1]), pk2(qa[2][tt][2], qa[2][tt][3])};
          ok[tt] = (u32x2){phi2(qa[1][tt][0], qa[1][tt][1]), phi2(qa[1][tt][2], qa[1][tt][3])};
        }
        SCHED();
#pragma unroll
        for (int tt = 0; tt < 4; ++tt) {
          *(LAS u32x2*)(size_t)(av + tt * 16 * 152) = ov[tt];
          *(LAS u32x2*)(size_t)(ak + tt * 16 * 144) = ok[tt];
        }
        if (w < 2) {
#pragma unroll
          for (int tt = 0; tt < 4; ++tt) orm[tt] = (u32x2){phi2(qa[3][tt][0], qa[3][tt][1]), phi2(qa[3][tt][2], qa[3][tt][3])};
        } else {
#pragma unroll
          for (int tt = 0; tt < 4; ++tt) {
            orm[tt] = (u32x2){pk2(qa[3][tt][0], qa[3][tt][1]), pk2(qa[3][tt][2], qa[3][tt][3])};
            if (w == 3) orm[tt] = (u32x2){0x00003f80u, 0u};
          }
        }
#pragma unroll
        for (int tt = 0; tt < 4; ++tt) oq[tt] = (u32x2){phi2(qa[0][tt][0], qa[0][tt][1]), phi2(qa[0][tt][2], qa[0][tt][3])};
        SCHED();
#pragma unroll
        for (int tt = 0; tt < 4; ++tt) {
          *(LAS u32x2*)(size_t)(ar + tt * rs) = orm[tt];
          *(LAS u32x2*)(size_t)(aq + tt * 16 * 144) = oq[tt];
        }
      }
      WAIT_LGKM(0);
      BAR();
      const unsigned ti = (unsigned)(0x4433322221111000ull >> (16 * w)) & 0xffffu;
      const unsigned tj = (unsigned)(0x4343243214210410ull >> (16 * w)) & 0xffffu;
      const unsigned kr = LB + KTMP + (8 * g4 + q) * 144 + p * 8, vr = LB + VTMP + (8 * g4 + q) * 152 + p * 8;
#pragma unroll
      for (int n = 0; n < 4; ++n) {
        const unsigned di = (ti >> (4 * n)) & 15u, ej = (tj >> (4 * n)) & 15u;
        f32x4 d = {0.f, 0.f, 0.f, 0.f};
#pragma unroll
        for (int ks = 0; ks < 2; ++ks) {
          const unsigned ka = kr + ks * 32 * 144 + di * 32, va = vr + ks * 32 * 152 + ej * 32;
          const bf16x8 fa = join(lds_tr(ka), lds_tr(ka + 4 * 144));
          const bf16x8 fb = join(lds_tr(va), lds_tr(va + 4 * 152));
          d = mfma(fa, fb, d);
        }
        __builtin_amdgcn_raw_buffer_store_b128(__builtin_bit_cast(u32x4, d), wsr,
                                               WS_P + (g * 32 + t) * 16384 + (4 * w + n) * 1024 + lane * 16, 0, AUX_SC1);
      }
#pragma unroll
      for (int i = 0; i < 9; ++i) {
        const f32x4 bv = {bf_lo(bb[i][0]), bf_hi(bb[i][0]), bf_lo(bb[i][1]), bf_hi(bb[i][1])};
#pragma unroll
        for (int j = 0; j < 4; ++j) acc[i][j] = bv;
      }
      WAIT_LGKM(0);
      BAR();
      STAMP(5);
    }
    bf16x8 A[9];
    {
#pragma unroll
      for (int j = 0; j < 4; ++j) B0[j] = lds_b128(b0 + j * 16 * ACT_STRIDE);
      A[0] = join(lds_tr(a1), lds_tr(a1 + 512));
      A[1] = join(lds_tr(a1 + 4096), lds_tr(a1 + 4096 + 512));
      A[2] = join(lds_tr(a1 + 8192), lds_tr(a1 + 8192 + 512));
      unsigned bc = b0;
#pragma unroll 1
      for (int it = 0; it < 9; ++it) {
        if (it == 3) { WAIT_VM(0); asm volatile("" ::"v"(dummy)); }
        kstep9<64>(acc, A, B0, B1, a1, a0, bc);
        kstep9<128>(acc, A, B1, B0, a0, a1, bc);
        bc += 128;
      }
      STAMP(6);
      const int lane = fresh_lane(), g4 = lane >> 4;
      const unsigned ha = LB + (lane & 15) * ACT_STRIDE + (16 * w + 8 * (g4 >> 1)) * 2;
      u32x4 hq[18];
#pragma unroll
      for (int n = 0; n < 18; ++n) {
        const f32x4 u = n < 16 ? acc[2 * (n & 3)][n >> 2] : acc[8][2 * (n - 16)];
        const f32x4 v = n < 16 ? acc[2 * (n & 3) + 1][n >> 2] : acc[8][2 * (n - 16) + 1];
        const auto s0 = __builtin_amdgcn_permlane16_swap(pk2v(relu2(u[0], u[1])), pk2v(relu2(v[0], v[1])), false, false);
        const auto s1 = __builtin_amdgcn_permlane16_swap(pk2v(relu2(u[2], u[3])), pk2v(relu2(v[2], v[3])), false, false);
        hq[n] = (u32x4){(unsigned)s0[0], (unsigned)s1[0], (unsigned)s0[1], (unsigned)s1[1]};
        if (n >= 2) {
          SCHED();
          const int m = n - 2;
          *(LAS u32x4*)(size_t)(ha + (m >> 2) * 16 * ACT_STRIDE + (128 * (m & 3) + 64 * (g4 & 1)) * 2) = hq[m];
          SCHED();
        }
      }
      *(LAS u32x4*)(size_t)(ha + (g4 & 1) * 16 * ACT_STRIDE + 512 * 2) = hq[16];
      *(LAS u32x4*)(size_t)(ha + (2 + (g4 & 1)) * 16 * ACT_STRIDE + 512 * 2) = hq[17];
      WAIT_LGKM(0);
      BAR();
      STAMP(7);
    }
    {
#pragma unroll
      for (int j = 0; j < 4; ++j) B0[j] = lds_b128(b0 + j * 16 * ACT_STRIDE);
#pragma unroll
      for (int i = 0; i < 9; ++i)
#pragma unroll
        for (int j = 0; j < 4; ++j) acc[i][j] = (f32x4){0.f, 0.f, 0.f, 0.f};
      unsigned bc = b0;
      kstep9<64>(acc, A, B0, B1, a1, a0, bc);
#pragma unroll 1
      for (int it = 0; it < 10; ++it) {
        if (it == 8) bc -= 18 * 64;
        kstep9<128>(acc, A, B1, B0, a0, a1, bc);
        kstep9<192>(acc, A, B0, B1, a1, a0, bc);
        bc += 128;
      }
    }
    STAMP(8);
    {
      const int lane = fresh_lane(), g4 = lane >> 4;
      const int hf = (lane >> 3) & 1, l32 = g4 * 8 + (lane & 7);
#pragma unroll
      for (int tt = 0; tt < 4; ++tt) {
        const bool own = 2 * tt + hf == g;
        const unsigned sb = own ? OOB : WS_SLAB + ((t * 8 + 2 * tt + hf) * 8 + g) * 9216;
#pragma unroll
        for (int j = 0; j < 4; ++j) {
          u32x4 o;
          o[0] = pk2(acc[2 * j][tt][0], acc[2 * j][tt][1]); o[1] = pk2(acc[2 * j][tt][2], acc[2 * j][tt][3]);
          o[2] = pk2(acc[2 * j + 1][tt][0], acc[2 * j + 1][tt][1]); o[3] = pk2(acc[2 * j + 1][tt][2], acc[2 * j + 1][tt][3]);
          __builtin_amdgcn_raw_buffer_store_b128(o, wsr, sb + ((w * 4 + j) * 32 + l32) * 16, 0, AUX_SC1);
        }
        u32x2 o2;
        o2[0] = pk2(acc[8][tt][0], acc[8][tt][1]); o2[1] = pk2(acc[8][tt][2], acc[8][tt][3]);
        __builtin_amdgcn_raw_buffer_store_b64(o2, wsr, sb + 8192 + (w * 32 + l32) * 8, 0, AUX_SC1);
        if (tt == (g >> 1) && own) {
          const unsigned fa = LB + (lane & 7) * FIN_STRIDE + (16 * w + 4 * g4) * 4;
#pragma unroll
          for (int blk = 0; blk < 9; ++blk) *(LAS f32x4*)(size_t)(fa + blk * 256) = acc[blk][tt];
        }
      }
    }
    STAMP(9);
    WAIT_VM(0);
    STAMP(10);
  }
  {
    int tid = threadIdx.x; asm volatile("" : "+v"(tid));
    const int c0 = tid, c1 = tid + 512;
    const unsigned short* xrow = x + (size_t)(t * 64 + g * 8) * DM;
    u32x4 xa = *(const u32x4*)(xrow + (size_t)c0 * 8), ba = *(const u32x4*)(b2 + (c0 % 72) * 8);
    u32x4 xb = xa, bb = ba;
    if (tid < 64) { xb = *(const u32x4*)(xrow + (size_t)c1 * 8); bb = *(const u32x4*)(b2 + (c1 % 72) * 8); }
    WAIT_LGKM(0);
    BAR();
    if (tid == 0) st_flag((unsigned*)(ws + WS_FLAG3) + t * 32 + g, tag);
    const unsigned sb = WS_SLAB + (t * 8 + g) * 8 * 9216;
    f32x4 lo = {0.f, 0.f, 0.f, 0.f}, hi = lo, m = lo;
    {
      const int lane = tid & 63;
      const unsigned* f3 = (const unsigned*)(ws + WS_FLAG3) + t * 32;
      const bool poll = lane < 8 && lane != g;
      unsigned f0 = poll ? ld_flag(f3 + lane) : tag;
#pragma unroll 1
      for (unsigned spins = 0; spins < SPIN_CAP; ++spins) {
        if (__all(f0 == tag)) break;
        __builtin_amdgcn_s_sleep(1);
        if (poll) f0 = ld_flag(f3 + lane);
      }
      asm volatile("" ::: "memory");
      STAMP(11);
      u32x4 v[7];
      u32x2 r[7];
#pragma unroll
      for (int s = 0; s < 7; ++s) {
        const unsigned so = sb + ((g + 1 + s) & 7) * 9216;
        v[s] = __builtin_amdgcn_raw_buffer_load_b128(wsr, so + tid * 16, 0, AUX_SC1);
        r[s] = __builtin_amdgcn_raw_buffer_load_b64(wsr, tid < 128 ? so + 8192 + tid * 8 : OOB, 0, AUX_SC1);
      }
#pragma unroll
      for (int s = 0; s < 7; ++s) {
        lo += unpk_lo(v[s]); hi += unpk_hi(v[s]);
        m += (f32x4){bf_lo(r[s][0]), bf_hi(r[s][0]), bf_lo(r[s][1]), bf_hi(r[s][1])};
      }
    }
    STAMP(12);
    const int cw = tid >> 7, cj = (tid >> 5) & 3, cg = (tid >> 3) & 3, tk = tid & 7;
    const unsigned fa = LB + tk * FIN_STRIDE + (128 * cj + 16 * cw + 4 * cg) * 4;
    *(LAS f32x4*)(size_t)fa += lo;
    *(LAS f32x4*)(size_t)(fa + 256) += hi;
    if (tid < 128) *(LAS f32x4*)(size_t)(LB + tk * FIN_STRIDE + (512 + 16 * (tid >> 5) + 4 * cg) * 4) += m;
    STAMP(13);
    __syncthreads();
    auto fin = [&](int c, u32x4 xv, u32x4 bv) {
      const unsigned fb = LB + (c / 72) * FIN_STRIDE + (c % 72) * 32;
      const f32x4 a = *(LAS f32x4*)(size_t)fb + unpk_lo(xv) + unpk_lo(bv), b = *(LAS f32x4*)(size_t)(fb + 16) + unpk_hi(xv) + unpk_hi(bv);
      const u32x4 o = {pk2(a[0], a[1]), pk2(a[2], a[3]), pk2(b[0], b[1]), pk2(b[2], b[3])};
      *(u32x4*)(out + (size_t)(t * 64 + g * 8) * DM + (size_t)c * 8) = o;
    };
    fin(c0, xa, ba);
    if (tid < 64) fin(c1, xb, bb);
    STAMP(14); STAMP_RT(21);
    STAMP_FLUSH();
    if (blockIdx.x == 0 && tid == 0) st_flag((unsigned*)(ws + WS_EPOCH), tag);
  }
}

extern "C" void kernel_launch(void* const* d_in, const int* in_sizes, int n_in, void* d_out, int out_size, void* d_ws,
                              size_t ws_size, hipStream_t stream) {
  (void)in_sizes; (void)n_in; (void)out_size; (void)ws_size;
  (void)hipFuncSetAttribute((const void*)fused_block_kernel, hipFuncAttributeMaxDynamicSharedMemorySize, LDS_BYTES);
  fused_block_kernel<<<256, 512, LDS_BYTES, stream>>>(
      (const unsigned short*)d_in[0], (const char*)d_in[1], (const char*)d_in[2], (const char*)d_in[4],
      (const unsigned short*)d_in[5], (const char*)d_in[6], (const unsigned short*)d_in[7], (unsigned short*)d_out, (char*)d_ws);
}
```

```cpp
#include <hip/hip_runtime.h>
#include <hip/hip_bf16.h>
#include <type_traits>

typedef __attribute__((ext_vector_type(4))) short s16x4;
typedef __attribute__((ext_vector_type(8))) __bf16 bf16x8;
typedef __attribute__((ext_vector_type(2))) __bf16 bf16x2;
typedef __attribute__((ext_vector_type(4))) float f32x4;
typedef __attribute__((ext_vector_type(2))) float f32x2;
typedef __attribute__((ext_vector_type(4))) unsigned u32x4;
typedef __attribute__((ext_vector_type(2))) unsigned u32x2;
#define LAS __attribute__((address_space(3)))
#define GAS __attribute__((address_space(1)))
#define DI __device__ __forceinline__
#define SCHED() __builtin_amdgcn_sched_barrier(0)
#define BAR() __builtin_amdgcn_s_barrier()
#define WAIT_VM(n) asm volatile("s_waitcnt vmcnt(%0)" ::"n"(n) : "memory")
#define WAIT_LGKM(n) asm volatile("s_waitcnt lgkmcnt(%0)" ::"n"(n) : "memory")

#define STAMP(i) do {} while (0)
#define STAMP_RT(i) do {} while (0)
#define STAMP_DECL
#define STAMP_FLUSH() do {} while (0)

#define XPOL ""
#define MAP_NGX 2

namespace {

constexpr int DM = 576;
constexpr int ACT_STRIDE = 1184;
constexpr int RING = 64 * ACT_STRIDE;
constexpr int KSLOT = 36864;
constexpr int PQ = RING + 2 * KSLOT;
constexpr int PQ_STRIDE = 144;
constexpr int SIMG = PQ + 64 * PQ_STRIDE;
constexpr int LDS_BYTES = 163840;
constexpr int KTMP = RING + 16384;
constexpr int VTMP = KTMP + 64 * 144;
static_assert(SIMG + 5120 <= LDS_BYTES, "lds");
static_assert(VTMP + 64 * 152 + 16 <= RING + KSLOT, "lds");

constexpr unsigned WS_EPOCH = 0;
constexpr unsigned WS_FLAG1 = 1024;
constexpr unsigned WS_FLAG2 = 2048;
constexpr unsigned WS_FLAG3 = 4096;
constexpr unsigned WS_P = 65536;
constexpr unsigned WS_S = WS_P + 256 * 16384;
constexpr unsigned WS_SLAB = WS_S + 8 * 8192;
constexpr unsigned WS_END = WS_SLAB + 32 * 8 * 8 * 9216;
constexpr unsigned OOB = 0x7ffffff0u;
constexpr int AUX_SC1 = 16;
constexpr unsigned SPIN_CAP = 1u << 16;

DI float bf_lo(unsigned u) { return __builtin_bit_cast(float, u << 16); }
DI float bf_hi(unsigned u) { return __builtin_bit_cast(float, u & 0xffff0000u); }
DI unsigned pk2(float a, float b) {
  f32x2 p = {a, b};
  return __builtin_bit_cast(unsigned, __builtin_convertvector(p, bf16x2));
}
DI float rbf(float a) { return bf_lo(pk2(a, a)); }
template <int CTRL> DI float dpp_add(float v) {
  return v + __builtin_bit_cast(float, __builtin_amdgcn_update_dpp(0, __builtin_bit_cast(int, v), CTRL, 0xf, 0xf, true));
}
DI int fresh_lane() { int v = threadIdx.x & 63; asm volatile("" : "+v"(v)); return v; }
DI bf16x8 lds_b128(unsigned addr) { return *(LAS bf16x8*)(size_t)addr; }
DI s16x4 lds_tr(unsigned addr) { return __builtin_amdgcn_ds_read_tr16_b64_v4i16((LAS s16x4*)(size_t)addr); }
DI bf16x8 join(s16x4 lo, s16x4 hi) {
  return __builtin_bit_cast(bf16x8, __builtin_shufflevector(lo, hi, 0, 1, 2, 3, 4, 5, 6, 7));
}
DI f32x4 mfma(bf16x8 a, bf16x8 b, f32x4 c) { return __builtin_amdgcn_mfma_f32_16x16x32_bf16(a, b, c, 0, 0, 0); }
DI float pos(float v) { const int b = __builtin_bit_cast(int, v); return __builtin_bit_cast(float, b > 0 ? b : 0); }
DI float neg(float v) { const int b = __builtin_bit_cast(int, v); return __builtin_bit_cast(float, b < 0 ? b : 0); }
DI f32x2 relu2(float a, float b) { const f32x2 p = {pos(a), pos(b)}; return p * p; }
DI unsigned phi2(float a, float b) {
  const f32x2 m = (f32x2){a, b} * 1.44269504088896f;
  const f32x2 e = {__builtin_amdgcn_exp2f(neg(m[0])), __builtin_amdgcn_exp2f(neg(m[1]))};
  const f32x2 r = ((f32x2){pos(a), pos(b)} + 1e-6f) + e;
  return __builtin_bit_cast(unsigned, __builtin_convertvector(r, bf16x2));
}
DI unsigned pk2v(f32x2 p) { return __builtin_bit_cast(unsigned, __builtin_convertvector(p, bf16x2)); }
DI unsigned ld_flag(const unsigned* p) { return __hip_atomic_load((GAS unsigned*)p, __ATOMIC_RELAXED, __HIP_MEMORY_SCOPE_AGENT); }
DI void st_flag(unsigned* p, unsigned v) { __hip_atomic_store((GAS unsigned*)p, v, __ATOMIC_RELAXED, __HIP_MEMORY_SCOPE_AGENT); }
DI void wait_flags(const unsigned* base, int count, unsigned tag, int lane, unsigned first) {
  unsigned v = first;
#pragma unroll 1
  for (unsigned spins = 0; spins < SPIN_CAP; ++spins) {
    if (__all(lane >= count || v == tag)) break;
    __builtin_amdgcn_s_sleep(2);
    if (lane < count) v = ld_flag(base + lane);
  }
  asm volatile("" ::: "memory");
}
DI f32x4 unpk_lo(u32x4 v) { return (f32x4){bf_lo(v[0]), bf_hi(v[0]), bf_lo(v[1]), bf_hi(v[1])}; }
DI f32x4 unpk_hi(u32x4 v) { return (f32x4){bf_lo(v[2]), bf_hi(v[2]), bf_lo(v[3]), bf_hi(v[3])}; }

DI void dma(unsigned voff, const char* sbase, unsigned m0v) {
  asm volatile("s_mov_b32 m0, %1\n\ts_nop 0\n\tglobal_load_lds_dwordx4 %0, %2" ::"v"(voff), "s"(m0v), "s"(sbase) : "memory");
}
template <int B, int P> DI void piece(const unsigned (&voff)[5], const char* sbase, unsigned m) {
  asm volatile("s_add_i32 m0, %1, %3\n\ts_nop 0\n\tglobal_load_lds_dwordx4 %0, %2 offset:%4"
               ::"v"(voff[P]), "s"(m), "s"(sbase), "n"(B * 4096 + P * 1024 - B * 128), "n"(B * 128) : "memory", "scc");
}
DI void touch(const char* sbase, unsigned voff, unsigned& dummy) { asm volatile("global_load_dword %0, %1, %2" : "+v"(dummy) : "v"(voff), "s"(sbase) : "memory"); }

template <int BOFF0>
DI void kstep9(f32x4 (&acc)[9][4], bf16x8 (&A)[9], const bf16x8 (&Bc)[4], bf16x8 (&Bn)[4], unsigned ab, unsigned abn, unsigned bn) {
  s16x4 lx, ly;
  constexpr int BTS = 16 * ACT_STRIDE;
#define MM(blk, j) acc[blk][j] = mfma(A[blk], Bc[j], acc[blk][j])
#define RL(v, base, blk) v = lds_tr((base) + (blk) * 4096)
#define RH(v, base, blk) A[blk] = join(v, lds_tr((base) + (blk) * 4096 + 512))
  RL(lx, ab, 3); SCHED(); MM(0, 0); SCHED(); RH(lx, ab, 3); SCHED(); MM(0, 1); SCHED(); MM(0, 2); MM(0, 3); SCHED();
  MM(1, 0); SCHED(); Bn[0] = lds_b128(bn + BOFF0); SCHED(); MM(1, 1); SCHED();
  Bn[1] = lds_b128(bn + BOFF0 + BTS); SCHED(); MM(1, 2); MM(1, 3); SCHED();
  WAIT_LGKM(2);
  BAR();
  RL(lx, ab, 4); SCHED(); MM(2, 0); SCHED(); RH(lx, ab, 4); SCHED(); MM(2, 1); SCHED();
  RL(ly, ab, 5); SCHED(); MM(2, 2); SCHED(); RH(ly, ab, 5); SCHED(); MM(2, 3); SCHED();
  RL(lx, ab, 6); SCHED(); MM(3, 0); SCHED(); RH(lx, ab, 6); SCHED(); MM(3, 1); SCHED(); MM(3, 2); MM(3, 3); SCHED();
  RL(lx, ab, 7); SCHED(); MM(4, 0); SCHED(); RH(lx, ab, 7); SCHED(); MM(4, 1); SCHED(); MM(4, 2); MM(4, 3); SCHED();
  RL(lx, ab, 8); SCHED(); MM(5, 0); SCHED(); RH(lx, ab, 8); SCHED(); MM(5, 1); SCHED(); MM(5, 2); MM(5, 3); SCHED();
  MM(6, 0); SCHED(); Bn[2] = lds_b128(bn + BOFF0 + 2 * BTS); SCHED(); MM(6, 1); SCHED();
  Bn[3] = lds_b128(bn + BOFF0 + 3 * BTS); SCHED(); MM(6, 2); MM(6, 3); SCHED();
  WAIT_LGKM(2);
  BAR();
  RL(lx, abn, 0); SCHED(); MM(7, 0); SCHED(); RH(lx, abn, 0); SCHED(); MM(7, 1); SCHED();
  RL(ly, abn, 1); SCHED(); MM(7, 2); SCHED(); RH(ly, abn, 1); SCHED(); MM(7, 3); SCHED();
  RL(lx, abn, 2); SCHED(); MM(8, 0); SCHED(); RH(lx, abn, 2); SCHED(); MM(8, 1); SCHED(); MM(8, 2); MM(8, 3); SCHED();
}
template <int BOFF0>
DI void kstep4(f32x4 (&acc)[4][4], bf16x8 (&A)[4], const bf16x8 (&Bc)[4], bf16x8 (&Bn)[4], unsigned ab, unsigned abn, unsigned d3, unsigned bn) {
  s16x4 lx, ly;
  constexpr int BTS = 16 * ACT_STRIDE;
#define QL(v, base, off) v = lds_tr((base) + (off))
#define QH(v, i, base, off) A[i] = join(v, lds_tr((base) + (off) + 576))
  const unsigned a3 = ab + d3;
  QL(lx, a3, 0); SCHED(); MM(0, 0); SCHED(); QH(lx, 3, a3, 0); SCHED(); MM(0, 1); SCHED();
  Bn[0] = lds_b128(bn + BOFF0); SCHED(); MM(0, 2); SCHED(); Bn[1] = lds_b128(bn + BOFF0 + BTS); SCHED(); MM(0, 3); SCHED();
  Bn[2] = lds_b128(bn + BOFF0 + 2 * BTS); SCHED(); MM(1, 0); SCHED(); Bn[3] = lds_b128(bn + BOFF0 + 3 * BTS); SCHED();
  MM(1, 1); MM(1, 2); MM(1, 3); SCHED();
  WAIT_LGKM(4);
  BAR();
  QL(lx, abn, 0); SCHED(); MM(2, 0); SCHED(); QH(lx, 0, abn, 0); SCHED(); MM(2, 1); SCHED();
  QL(ly, abn, 5120); SCHED(); MM(2, 2); SCHED(); QH(ly, 1, abn, 5120); SCHED(); MM(2, 3); SCHED();
  QL(lx, abn, 10240); SCHED(); MM(3, 0); SCHED(); QH(lx, 2, abn, 10240); SCHED(); MM(3, 1); MM(3, 2); MM(3, 3); SCHED();
}
#undef MM

}

__global__ __launch_bounds__(512, 2) void fused_block_kernel(
    const unsigned short* __restrict__ x, const char* __restrict__ wqkv, const char* __restrict__ wo,
    const char* __restrict__ w1, const unsigned short* __restrict__ b1, const char* __restrict__ w2,
    const unsigned short* __restrict__ b2, unsigned short* __restrict__ out, char* __restrict__ ws) {
  extern __shared__ __attribute__((aligned(16))) char lds[];
  const int tid = threadIdx.x, lane = tid & 63;
  const int wid = __builtin_amdgcn_readfirstlane(tid >> 6);
  constexpr int NGX = MAP_NGX, PFN = 32 / NGX;
  const int xj = blockIdx.x & 7, xi = blockIdx.x >> 3;
  const int g = NGX * (xj / NGX) + xi % NGX, pfr = xi / NGX, t = (xj % NGX) * PFN + pfr;
  const unsigned LB = (unsigned)(size_t)lds;
  const __amdgpu_buffer_rsrc_t wsr = __builtin_amdgcn_make_buffer_rsrc(ws, 0, WS_END, 0x00020000);

  STAMP_DECL
  STAMP(0); STAMP_RT(20);
  const int lr = lane >> 3, lc = lane & 7;
  const int l = wid - 4;
  const int cE = lc ^ (((lr >> 1) & 1) << 1), cO = cE ^ 4;
  unsigned voff[5];
  const unsigned m0r = LB + RING;
  const char* baseQ = wqkv + g * 144 + l * 1152;
  auto issueQ = [&](const char* sq, unsigned mq) {
    dma(voff[0], sq, mq); dma(voff[1], sq, mq + 1024); dma(voff[2], sq, mq + 2048); dma(voff[3], sq, mq + 3072); dma(voff[4], sq, mq + 4096);
  };
  unsigned dummy = 0;
  unsigned epoch;
  if (wid < 4) {
    u32x4 xr[2][9];
    const char* xp = (const char*)x + (size_t)(t * 64 + wid * 16) * 1152;
    const unsigned xo = lr * 1152 + lc * 16;
#pragma unroll
    for (int h = 0; h < 2; ++h)
#pragma unroll
      for (int i = 0; i < 9; ++i)
        asm volatile("global_load_dwordx4 %0, %1, %2 offset:%3 " XPOL : "=v"(xr[h][i]) : "v"(xo + h * 8 * 1152), "s"(xp), "n"(i * 128) : "memory");
    asm volatile("global_load_dword %0, %1, %2 sc1" : "=v"(epoch) : "v"(0u), "s"(ws) : "memory");
#pragma unroll
    for (int h = 0; h < 2; ++h) {
      if (h == 0) WAIT_VM(10); else WAIT_VM(0);
      SCHED();
#pragma unroll
      for (int i = 0; i < 9; ++i) asm volatile("" : "+v"(xr[h][i]));
      if (h == 0) STAMP(1);
      if (h == 1) {
        constexpr int RW1 = 576 / PFN, NP1 = (RW1 * 9 + 255) / 256;
        {
          const char* p1 = w1 + (size_t)(RW1 * pfr) * 9216 + g * 1152;
#pragma unroll
          for (int n = 0; n < NP1; ++n) {
            const int i0 = (4 * n + wid) * 64 + lane, i = i0 < RW1 * 9 ? i0 : 0;
            const int r = (i * 7282) >> 16;
            touch(p1, r * 9216 + (i - 9 * r) * 128, dummy);
          }
        }
      }
      const bf16x2 ones = __builtin_bit_cast(bf16x2, 0x3f803f80u);
      float s0 = 0.f, s1 = 0.f, q0 = 0.f, q1 = 0.f;
#pragma unroll
      for (int i = 0; i < 9; ++i) {
        const unsigned u0 = xr[h][i][0], u1 = xr[h][i][1], u2 = xr[h][i][2], u3 = xr[h][i][3];
        const bf16x2 a = __builtin_bit_cast(bf16x2, u0), b = __builtin_bit_cast(bf16x2, u1);
        const bf16x2 c = __builtin_bit_cast(bf16x2, u2), d = __builtin_bit_cast(bf16x2, u3);
        s0 = __builtin_amdgcn_fdot2_f32_bf16(a, ones, s0, false); s1 = __builtin_amdgcn_fdot2_f32_bf16(b, ones, s1, false);
        q0 = __builtin_amdgcn_fdot2_f32_bf16(a, a, q0, false); q1 = __builtin_amdgcn_fdot2_f32_bf16(b, b, q1, false);
        s0 = __builtin_amdgcn_fdot2_f32_bf16(c, ones, s0, false); s1 = __builtin_amdgcn_fdot2_f32_bf16(d, ones, s1, false);
        q0 = __builtin_amdgcn_fdot2_f32_bf16(c, c, q0, false); q1 = __builtin_amdgcn_fdot2_f32_bf16(d, d, q1, false);
      }
      float s = s0 + s1, q = q0 + q1;
      s = dpp_add<0xB1>(s); q = dpp_add<0xB1>(q);
      s = dpp_add<0x4E>(s); q = dpp_add<0x4E>(q);
      s = dpp_add<0x141>(s); q = dpp_add<0x141>(q);
      const float mean = s * (1.f / 576.f);
      const float var = fmaxf(q * (1.f / 576.f) - mean * mean, 0.f);
      const float mb = rbf(mean);
      const float sd = rbf(__builtin_amdgcn_sqrtf(rbf(rbf(var) + 1e-5f)));
      const float rs = __builtin_amdgcn_rcpf(sd), nb = -mb * rs;
      const unsigned wa = LB + (wid * 16 + h * 8 + lr) * ACT_STRIDE + lc * 16;
#pragma unroll
      for (int i = 0; i < 9; ++i) {
        u32x4 o;
#pragma unroll
        for (int e = 0; e < 4; ++e) o[e] = pk2(fmaf(bf_lo(xr[h][i][e]), rs, nb), fmaf(bf_hi(xr[h][i][e]), rs, nb));
        *(LAS u32x4*)(size_t)(wa + i * 128) = o;
      }
    }
  } else {
    asm volatile("global_load_dword %0, %1, %2 sc1" : "=v"(epoch) : "v"(0u), "s"(ws) : "memory");
    if (wid == 7) {
      constexpr int RW = 576 / PFN, NQ = (RW + 7) / 8;
      const char* pq_ = wqkv + (size_t)(RW * pfr) * 3456 + ((g * 144) & ~127);
      const int c6 = lc < 5 ? lc : 5;
#pragma unroll
      for (int n = 0; n < NQ; ++n) {
        const int r = 8 * n + lr < RW ? 8 * n + lr : RW - 1;
        touch(pq_, r * 3456 + (c6 >> 1) * 1152 + (c6 & 1) * 128, dummy);
      }
      WAIT_VM(NQ);
    } else {
#pragma unroll
      for (int p = 0; p < 5; ++p) {
        int i = 64 * p + lane; i = i < 288 ? i : 287;
        const int r = (i * 7282) >> 16;
        voff[p] = r * 3456 + (i - 9 * r) * 16;
      }
      issueQ(baseQ, m0r + l * 5120);
      WAIT_VM(5);
    }
    SCHED();
    STAMP(1);
  }
  asm volatile("" : "+v"(epoch));
  const unsigned tag = __builtin_amdgcn_readfirstlane(epoch) + 1u;
  STAMP(2);

  if (wid >= 4) {
#define LPRIO 2
#define CPRIO 3
    __builtin_amdgcn_s_setprio(LPRIO);
    const char* base1 = w1 + g * 1152;
    const char* base2 = w2 + (size_t)(g * 576) * 1152;
    const char* baseO = wo + (size_t)(g * 72) * 1152;
    auto setvoff = [&](unsigned ldw, int rowsel) {
#pragma unroll
      for (int p = 0; p < 4; ++p) voff[p] = (rowsel * 8 * p + lr) * ldw + ((p & 1) ? cO : cE) * 16;
    };
    using I1 = std::integral_constant<int, 1>;
    WAIT_LGKM(0);
    if (l < 3) {
      auto prog = [&](auto LC) {
        constexpr int L = decltype(LC)::value;
        auto issue8 = [&](const char* sb, unsigned m) {
          if constexpr (L == 0) { piece<8, 0>(voff, sb, m); piece<8, 3>(voff, sb, m); }
          if constexpr (L == 1) piece<8, 1>(voff, sb, m);
          if constexpr (L == 2) piece<8, 2>(voff, sb, m);
        };
        auto issueLo = [&](const char* sb, unsigned m) {
          if constexpr (L == 0) { piece<0, 2>(voff, sb, m); piece<1, 1>(voff, sb, m); piece<2, 0>(voff, sb, m); piece<2, 3>(voff, sb, m); piece<3, 2>(voff, sb, m); }
          if constexpr (L == 1) { piece<0, 0>(voff, sb, m); piece<0, 3>(voff, sb, m); piece<1, 2>(voff, sb, m); piece<2, 1>(voff, sb, m); piece<3, 0>(voff, sb, m); piece<3, 3>(voff, sb, m); }
          if constexpr (L == 2) { piece<0, 1>(voff, sb, m); piece<1, 0>(voff, sb, m); piece<1, 3>(voff, sb, m); piece<2, 2>(voff, sb, m); piece<3, 1>(voff, sb, m); }
        };
        auto issueHi = [&](const char* sb, unsigned m) {
          if constexpr (L == 0) { piece<4, 1>(voff, sb, m); piece<5, 0>(voff, sb, m); piece<5, 3>(voff, sb, m); piece<6, 2>(voff, sb, m); piece<7, 1>(voff, sb, m); }
          if constexpr (L == 1) { piece<4, 2>(voff, sb, m); piece<5, 1>(voff, sb, m); piece<6, 0>(voff, sb, m); piece<6, 3>(voff, sb, m); piece<7, 2>(voff, sb, m); }
          if constexpr (L == 2) { piece<4, 0>(voff, sb, m); piece<4, 3>(voff, sb, m); piece<5, 2>(voff, sb, m); piece<6, 1>(voff, sb, m); piece<7, 0>(voff, sb, m); piece<7, 3>(voff, sb, m); }
        };
        constexpr int WX = L == 0 ? 10 : 11;
        const unsigned mA = m0r, mB = m0r + KSLOT;
        {
          const char* sq = baseQ + 32 * 3456;
          const unsigned mq = m0r + L * 5120;
          issueQ(sq, mq + 16384); issueQ(sq + 32 * 3456, mq + KSLOT);
          sq += 64 * 3456;
          STAMP(15);
          WAIT_VM(10);
          STAMP(16);
          BAR();
          STAMP(3);
          issueQ(sq, mq + KSLOT + 16384); WAIT_VM(10); BAR();
          sq += 32 * 3456;
#pragma unroll 1
          for (int j = 0; j < 3; ++j) {
            issueQ(sq, mq); WAIT_VM(10); BAR();
            issueQ(sq + 32 * 3456, mq + 16384); WAIT_VM(10); BAR();
            issueQ(sq + 64 * 3456, mq + KSLOT); WAIT_VM(10); BAR();
            issueQ(sq + 96 * 3456, mq + KSLOT + 16384); WAIT_VM(10); BAR();
            sq += 128 * 3456;
          }
          issueQ(sq, mq); WAIT_VM(10); BAR();
          issueQ(sq + 32 * 3456, mq + 16384); WAIT_VM(10); BAR();
        }
        setvoff(9216, 1);
        const char* sb = base1;
        issueLo(sb, mB); WAIT_VM(10); BAR();
        issueHi(sb, mB); issue8(sb, mB); WAIT_VM(12); BAR();
        issueLo(sb + 32 * 9216, mA); BAR(); BAR();
        WAIT_VM(12); BAR();
        auto kstepL = [&](const char* s1, unsigned m1, const char* s2, unsigned m2, auto mid) {
          issueHi(s1, m1); mid(); WAIT_VM(WX); BAR();
          issue8(s1, m1); issueLo(s2, m2); WAIT_VM(12); BAR();
        };
        auto nop = []() {};
        sb = base1 + 32 * 9216;
#pragma unroll 1
        for (int it = 0; it < 8; ++it) {
          kstepL(sb, mA, sb + 32 * 9216, mB, nop);
          kstepL(sb + 32 * 9216, mB, sb + 64 * 9216, mA, nop);
          sb += 64 * 9216;
        }
        issueHi(sb, mA); WAIT_VM(WX); BAR();
        issue8(sb, mA); setvoff(1152, 1); issueLo(base2, mB); WAIT_VM(12); BAR();
        sb = base2;
        kstepL(sb, mB, sb + 32 * 1152, mA, nop);
        kstepL(sb + 32 * 1152, mA, sb + 64 * 1152, mB, [&]() { BAR(); });
        sb += 64 * 1152;
#pragma unroll 1
        for (int it = 0; it < 7; ++it) {
          kstepL(sb, mB, sb + 32 * 1152, mA, nop);
          kstepL(sb + 32 * 1152, mA, sb + 64 * 1152, mB, nop);
          sb += 64 * 1152;
        }
        kstepL(sb, mB, sb + 32 * 1152, mA, nop);
        sb -= 128 * 1152;
        kstepL(sb + 160 * 1152, mA, baseO, mB, nop);
        kstepL(baseO, mB, baseO + 32 * 1152, mA, nop);
        issueHi(baseO + 32 * 1152, mA); WAIT_VM(WX); BAR();
        issue8(baseO + 32 * 1152, mA); setvoff(1152, 0); issueLo(baseO + 64 * 1152, mB); WAIT_VM(12); BAR();
        issueHi(baseO + 64 * 1152, mB); WAIT_VM(WX); BAR();
        issue8(baseO + 64 * 1152, mB); WAIT_VM(6); BAR();
        WAIT_VM(0); BAR(); BAR();
      };
      if (l == 0) prog(std::integral_constant<int, 0>{});
      else if (l == 1) prog(std::integral_constant<int, 1>{});
      else prog(std::integral_constant<int, 2>{});
    } else {
      auto bars = [&](int n) {
#pragma unroll 1
        for (int i = 0; i < n; ++i) BAR();
      };
      const int lane = fresh_lane();
      constexpr int RW = 576 / PFN;
      BAR();
      STAMP(3);
      bars(17 + 1);
      {
        const char* p2 = base2 + (size_t)(RW * pfr) * 1152;
#pragma unroll
        for (int n = 0; n < (RW * 9 + 63) / 64; ++n) {
          const int i = lane + 64 * n < RW * 9 ? lane + 64 * n : 0;
          touch(p2, i * 128, dummy);
        }
        constexpr int WOL = (648 + PFN - 1) / PFN;
        static_assert(WOL <= 64, "prefetch steps");
        int i = pfr * WOL + lane; i = (lane < WOL && i < 648) ? i : 0;
        touch(baseO, i * 128, dummy);
      }
      bars(2);
      bars(13);
      if (lane == 0) st_flag((unsigned*)(ws + WS_FLAG1) + g * 32 + t, tag);
      bars(7);
      unsigned fl = lane < 32 ? ld_flag((const unsigned*)(ws + WS_FLAG1) + g * 32 + lane) : tag;
      bars(3);
      STAMP(16);
      wait_flags((const unsigned*)(ws + WS_FLAG1) + g * 32, 32, tag, lane, fl);
      STAMP(17);
      {
        u32x4 hv[16];
        const unsigned po = WS_P + (g * 32 + (lane >> 5)) * 16384 + t * 512 + (lane & 31) * 16;
#pragma unroll
        for (int n = 0; n < 16; ++n)
          hv[n] = __builtin_amdgcn_raw_buffer_load_b128(wsr, po, n * 32768, AUX_SC1);
        SCHED();
        bars(5);
        f32x4 a = __builtin_bit_cast(f32x4, hv[0]), b = __builtin_bit_cast(f32x4, hv[1]);
#pragma unroll
        for (int n = 2; n < 16; n += 2) { a += __builtin_bit_cast(f32x4, hv[n]); b += __builtin_bit_cast(f32x4, hv[n + 1]); }
        a += b;
        SCHED();
        BAR();
        const int hh = lane >> 5, li = 32 * (t & 1) + (lane & 31), tile = t >> 1;
        float s0 = hh ? a[2] : a[0], s1 = hh ? a[3] : a[1], o0 = hh ? a[0] : a[2], o1 = hh ? a[1] : a[3];
        const unsigned x0 = (unsigned)__builtin_amdgcn_permlane32_swap(__builtin_bit_cast(unsigned, o0), __builtin_bit_cast(unsigned, o0), false, false)[hh ? 0 : 1];
        const unsigned x1 = (unsigned)__builtin_amdgcn_permlane32_swap(__builtin_bit_cast(unsigned, o1), __builtin_bit_cast(unsigned, o1), false, false)[hh ? 0 : 1];
        s0 += __builtin_bit_cast(float, x0); s1 += __builtin_bit_cast(float, x1);
        const int di = (int)((0x4433322221111000ull >> (4 * tile)) & 15u), ej = (int)((0x4343243214210410ull >> (4 * tile)) & 15u);
        const int d = 16 * di + 4 * (li >> 4) + 2 * hh, e = 16 * ej + (li & 15);
        const int hd = (d * 57) >> 10, he = (e * 57) >> 10;
        const bool ok = d < 72 && (e == 72 || (e < 72 && hd == he));
        const int row = e == 72 ? 18 : e - 18 * he;
        __builtin_amdgcn_raw_buffer_store_b32(pk2(s0, s1), wsr, ok ? WS_S + g * 8192 + hd * 1280 + row * 64 + (d - 18 * hd) * 2 : OOB, 0, AUX_SC1);
      }
      bars(5);
      WAIT_VM(0);
      if (lane == 0) st_flag((unsigned*)(ws + WS_FLAG2) + g * 32 + t, tag);
      bars(2 + 1 + 7);
      fl = lane < 32 ? ld_flag((const unsigned*)(ws + WS_FLAG2) + g * 32 + lane) : tag;
      bars(3);
      STAMP(18);
      wait_flags((const unsigned*)(ws + WS_FLAG2) + g * 32, 32, tag, lane, fl);
      STAMP(19);
      {
        u32x4 iv[5];
#pragma unroll
        for (int n = 0; n < 5; ++n) iv[n] = __builtin_amdgcn_raw_buffer_load_b128(wsr, WS_S + g * 8192 + lane * 16, n * 1024, AUX_SC1);
        SCHED();
        bars(5);
        const int c = lane & 3;
#pragma unroll
        for (int n = 0; n < 5; ++n) {
          const int row = 16 * n + (lane >> 2);
          const bool live = row != 19 && row != 39 && row != 59 && row != 79 && c < 3;
          u32x4 v = iv[n];
          v[0] = live ? v[0] : 0u;
          v[1] = (live && c < 2) ? v[1] : 0u; v[2] = (live && c < 2) ? v[2] : 0u; v[3] = (live && c < 2) ? v[3] : 0u;
          *(LAS u32x4*)(size_t)(LB + SIMG + n * 1024 + lane * 16) = v;
        }
        WAIT_LGKM(0);
      }
      bars(27);
    }
    STAMP(8);
    WAIT_VM(0);
    asm volatile("" ::"v"(dummy));
  } else {
    const int w = wid;
    __builtin_amdgcn_s_setprio(CPRIO);
    const int g4 = lane >> 4, q = (lane & 15) >> 2, p = lane & 3;
    const int cf = (((q >> 1) & 1) << 1) | ((g4 & 1) << 2);
    unsigned a0 = LB + RING + (8 * g4 + q) * 128 + (((2 * w + (p >> 1)) ^ cf) * 16) + (p & 1) * 8;
    unsigned a1 = a0 + KSLOT;
    asm volatile("" : "+v"(a1));
    const unsigned b0 = LB + (lane & 15) * ACT_STRIDE + g4 * 16;
    bf16x8 B0[4], B1[4];
    f32x4 acc[9][4];
    {
      f32x4 qa[4][4];
      bf16x8 A[4];
#pragma unroll
      for (int i = 0; i < 4; ++i)
#pragma unroll
        for (int j = 0; j < 4; ++j) qa[i][j] = (f32x4){0.f, 0.f, 0.f, 0.f};
      STAMP(15);
      WAIT_LGKM(0);
      BAR();
      STAMP(3);
#pragma unroll
      for (int j = 0; j < 4; ++j) B0[j] = lds_b128(b0 + j * 16 * ACT_STRIDE);
      unsigned ac = LB + RING + (8 * g4 + q) * 144 + 32 * w + 8 * p, an = ac + KSLOT, bc = b0;
      const unsigned d3 = (w < 3 ? w : 0) * 5120 + 128 - 32 * w;
      A[0] = join(lds_tr(ac), lds_tr(ac + 576));
      A[1] = join(lds_tr(ac + 5120), lds_tr(ac + 5120 + 576));
      A[2] = join(lds_tr(ac + 10240), lds_tr(ac + 10240 + 576));
#pragma unroll 1
      for (int it = 0; it < 9; ++it) {
        kstep4<64>(qa, A, B0, B1, ac, ac + 16384, d3, bc);
        kstep4<128>(qa, A, B1, B0, ac + 16384, an, d3, bc);
        const unsigned tmp = ac; ac = an; an = tmp;
        bc += 128;
      }
      STAMP(4);
      const int lane = fresh_lane(), g4 = lane >> 4, q = (lane & 15) >> 2, p = lane & 3;
      u32x2 bb[9];
#pragma unroll
      for (int blk = 0; blk < 9; ++blk) bb[blk] = *(const u32x2*)(b1 + g * 576 + 64 * blk + 16 * w + 4 * g4);
      {
        const unsigned tk = (lane & 15), col = (16 * w + 4 * g4) * 2;
        const unsigned aq = LB + PQ + tk * 144 + col, ak = LB + KTMP + tk * 144 + col, av = LB + VTMP + tk * 152 + col;
        constexpr unsigned DROP = 0x40000;
        const unsigned ar = w == 3 ? (g4 == 0 ? LB + VTMP + tk * 152 + 144 : DROP)
                                   : g4 >= 2 ? DROP : (w == 0 ? LB + PQ + tk * 144 : w == 1 ? LB + KTMP + tk * 144 : LB + VTMP + tk * 152) + 128 + g4 * 8;
        const unsigned rs = w >= 2 ? 16 * 152 : 16 * 144;
        u32x2 oq[4], ok[4], ov[4], orm[4];
#pragma unroll
        for (int tt = 0; tt < 4; ++tt) {
          ov[tt] = (u32x2){pk2(qa[2][tt][0], qa[2][tt][1]), pk2(qa[2][tt][2], qa[2][tt][3])};
          ok[tt] = (u32x2){phi2(qa[1][tt][0], qa[1][tt][1]), phi2(qa[1][tt][2], qa[1][tt][3])};
        }
        SCHED();
#pragma unroll
        for (int tt = 0; tt < 4; ++tt) {
          *(LAS u32x2*)(size_t)(av + tt * 16 * 152) = ov[tt];
          *(LAS u32x2*)(size_t)(ak + tt * 16 * 144) = ok[tt];
        }
        if (w < 2) {
#pragma unroll
          for (int tt = 0; tt < 4; ++tt) orm[tt] = (u32x2){phi2(qa[3][tt][0], qa[3][tt][1]), phi2(qa[3][tt][2], qa[3][tt][3])};
        } else {
#pragma unroll
          for (int tt = 0; tt < 4; ++tt) {
            orm[tt] = (u32x2){pk2(qa[3][tt][0], qa[3][tt][1]), pk2(qa[3][tt][2], qa[3][tt][3])};
            if (w == 3) orm[tt] = (u32x2){0x00003f80u, 0u};
          }
        }
#pragma unroll
        for (int tt = 0; tt < 4; ++tt) oq[tt] = (u32x2){phi2(qa[0][tt][0], qa[0][tt][1]), phi2(qa[0][tt][2], qa[0][tt][3])};
        SCHED();
#pragma unroll
        for (int tt = 0; tt < 4; ++tt) {
          *(LAS u32x2*)(size_t)(ar + tt * rs) = orm[tt];
          *(LAS u32x2*)(size_t)(aq + tt * 16 * 144) = oq[tt];
        }
      }
      WAIT_LGKM(0);
      BAR();
      const unsigned ti = (unsigned)(0x4433322221111000ull >> (16 * w)) & 0xffffu;
      const unsigned tj = (unsigned)(0x4343243214210410ull >> (16 * w)) & 0xffffu;
      const unsigned kr = LB + KTMP + (8 * g4 + q) * 144 + p * 8, vr = LB + VTMP + (8 * g4 + q) * 152 + p * 8;
#pragma unroll
      for (int n = 0; n < 4; ++n) {
        const unsigned di = (ti >> (4 * n)) & 15u, ej = (tj >> (4 * n)) & 15u;
        f32x4 d = {0.f, 0.f, 0.f, 0.f};
#pragma unroll
        for (int ks = 0; ks < 2; ++ks) {
          const unsigned ka = kr + ks * 32 * 144 + di * 32, va = vr + ks * 32 * 152 + ej * 32;
          const bf16x8 fa = join(lds_tr(ka), lds_tr(ka + 4 * 144));
          const bf16x8 fb = join(lds_tr(va), lds_tr(va + 4 * 152));
          d = mfma(fa, fb, d);
        }
        __builtin_amdgcn_raw_buffer_store_b128(__builtin_bit_cast(u32x4, d), wsr,
                                               WS_P + (g * 32 + t) * 16384 + (4 * w + n) * 1024 + lane * 16, 0, AUX_SC1);
      }
#pragma unroll
      for (int i = 0; i < 9; ++i) {
        const f32x4 bv = {bf_lo(bb[i][0]), bf_hi(bb[i][0]), bf_lo(bb[i][1]), bf_hi(bb[i][1])};
#pragma unroll
        for (int j = 0; j < 4; ++j) acc[i][j] = bv;
      }
      WAIT_LGKM(0);
      BAR();
      STAMP(5);
    }
    bf16x8 A[9];
    {
#pragma unroll
      for (int j = 0; j < 4; ++j) B0[j] = lds_b128(b0 + j * 16 * ACT_STRIDE);
      A[0] = join(lds_tr(a1), lds_tr(a1 + 512));
      A[1] = join(lds_tr(a1 + 4096), lds_tr(a1 + 4096 + 512));
      A[2] = join(lds_tr(a1 + 8192), lds_tr(a1 + 8192 + 512));
      unsigned bc = b0;
#pragma unroll 1
      for (int it = 0; it < 9; ++it) {
        if (__builtin_expect(it == 3, 0)) { WAIT_VM(0); asm volatile("" ::"v"(dummy)); }
        kstep9<64>(acc, A, B0, B1, a1, a0, bc);
        kstep9<128>(acc, A, B1, B0, a0, a1, bc);
        bc += 128;
      }
      STAMP(6);
      const int lane = fresh_lane(), g4 = lane >> 4;
      const unsigned ha = LB + (lane & 15) * ACT_STRIDE + (16 * w + 8 * (g4 >> 1)) * 2;
      u32x4 hq[18];
#pragma unroll
      for (int n = 0; n < 18; ++n) {
        const f32x4 u = n < 16 ? acc[2 * (n & 3)][n >> 2] : acc[8][2 * (n - 16)];
        const f32x4 v = n < 16 ? acc[2 * (n & 3) + 1][n >> 2] : acc[8][2 * (n - 16) + 1];
        const auto s0 = __builtin_amdgcn_permlane16_swap(pk2v(relu2(u[0], u[1])), pk2v(relu2(v[0], v[1])), false, false);
        const auto s1 = __builtin_amdgcn_permlane16_swap(pk2v(relu2(u[2], u[3])), pk2v(relu2(v[2], v[3])), false, false);
        hq[n] = (u32x4){(unsigned)s0[0], (unsigned)s1[0], (unsigned)s0[1], (unsigned)s1[1]};
        if (n >= 2) {
          SCHED();
          const int m = n - 2;
          *(LAS u32x4*)(size_t)(ha + (m >> 2) * 16 * ACT_STRIDE + (128 * (m & 3) + 64 * (g4 & 1)) * 2) = hq[m];
          SCHED();
        }
      }
      *(LAS u32x4*)(size_t)(ha + (g4 & 1) * 16 * ACT_STRIDE + 512 * 2) = hq[16];
      *(LAS u32x4*)(size_t)(ha + (2 + (g4 & 1)) * 16 * ACT_STRIDE + 512 * 2) = hq[17];
      WAIT_LGKM(0);
      BAR();
      STAMP(7);
    }
    {
#pragma unroll
      for (int j = 0; j < 4; ++j) B0[j] = lds_b128(b0 + j * 16 * ACT_STRIDE);
#pragma unroll
      for (int i = 0; i < 9; ++i)
#pragma unroll
        for (int j = 0; j < 4; ++j) acc[i][j] = (f32x4){0.f, 0.f, 0.f, 0.f};
      unsigned bc = b0;
      kstep9<64>(acc, A, B0, B1, a1, a0, bc);
#pragma unroll 1
      for (int it = 0; it < 10; ++it) {
        if (__builtin_expect(it == 6, 0)) {
          const int lane = fresh_lane(), g4 = lane >> 4, r16 = lane & 15;
          const unsigned sa = LB + SIMG + w * 1280 + g4 * 16;
          const bf16x8 ka0 = lds_b128(sa + r16 * 64), ka1 = lds_b128(sa + (r16 < 3 ? 16 + r16 : 19) * 64);
          const unsigned pa = LB + PQ + r16 * PQ_STRIDE + w * 36 + g4 * 16;
          const unsigned oa = LB + r16 * ACT_STRIDE + w * 36 + g4 * 8;
          const unsigned la = g4 == 0 ? oa + 32 : LB + r16 * ACT_STRIDE + 144 + w * 12 + (g4 - 1) * 4;
          u32x4 pb[4];
#pragma unroll
          for (int tt = 0; tt < 4; ++tt)
#pragma unroll
            for (int m = 0; m < 4; ++m) pb[tt][m] = *(LAS unsigned*)(size_t)(pa + tt * 16 * PQ_STRIDE + 4 * m);
#pragma unroll
          for (int tt = 0; tt < 4; ++tt) {
            const bf16x8 b = __builtin_bit_cast(bf16x8, pb[tt]);
            const f32x4 n0 = mfma(ka0, b, (f32x4){0.f, 0.f, 0.f, 0.f}), n1 = mfma(ka1, b, (f32x4){0.f, 0.f, 0.f, 0.f});
            const float d2 = n1[2];
            const unsigned du = __builtin_bit_cast(unsigned, d2);
            const unsigned y = (unsigned)__builtin_amdgcn_permlane32_swap(du, du, false, false)[0];
            const unsigned z = (unsigned)__builtin_amdgcn_permlane16_swap(y, y, false, false)[0];
            const float r = __builtin_amdgcn_rcpf(__builtin_bit_cast(float, z) + 1e-6f);
            const f32x2 p0 = (f32x2){n0[0], n0[1]} * r, p1 = (f32x2){n0[2], n0[3]} * r, p2 = (f32x2){n1[0], n1[1]} * r;
            *(LAS unsigned*)(size_t)(oa + tt * 16 * ACT_STRIDE) = pk2v(p0);
            *(LAS unsigned*)(size_t)(oa + tt * 16 * ACT_STRIDE + 4) = pk2v(p1);
            *(LAS unsigned*)(size_t)(la + tt * 16 * ACT_STRIDE) = g4 == 0 ? pk2v(p2) : 0u;
          }
        }
        kstep9<128>(acc, A, B1, B0, a0, a1, bc);
        kstep9<192>(acc, A, B0, B1, a1, a0, bc);
        bc += it == 7 ? 128 - 18 * 64 : 128;
      }
    }
    STAMP(8);
    {
      const int lane = fresh_lane(), g4 = lane >> 4;
      const int hf = (lane >> 3) & 1, l32 = g4 * 8 + (lane & 7);
#pragma unroll
      for (int tq = 0; tq < 4; ++tq) {
        const int tt = tq;
        const bool own = 2 * tt + hf == g;
        const unsigned sb = own ? OOB : WS_SLAB + ((t * 8 + 2 * tt + hf) * 8 + g) * 9216;
        u32x4 o[4];
#pragma unroll
        for (int j = 0; j < 4; ++j) {
          o[j][0] = pk2(acc[2 * j][tt][0], acc[2 * j][tt][1]); o[j][1] = pk2(acc[2 * j][tt][2], acc[2 * j][tt][3]);
          o[j][2] = pk2(acc[2 * j + 1][tt][0], acc[2 * j + 1][tt][1]); o[j][3] = pk2(acc[2 * j + 1][tt][2], acc[2 * j + 1][tt][3]);
        }
        const u32x2 o2 = {pk2(acc[8][tt][0], acc[8][tt][1]), pk2(acc[8][tt][2], acc[8][tt][3])};
#pragma unroll
        for (int j = 0; j < 4; ++j) __builtin_amdgcn_raw_buffer_store_b128(o[j], wsr, sb + ((w * 4 + j) * 32 + l32) * 16, 0, AUX_SC1);
        __builtin_amdgcn_raw_buffer_store_b64(o2, wsr, sb + 8192 + (w * 32 + l32) * 8, 0, AUX_SC1);
        if (tt == (g >> 1)) {
          const unsigned fa = own ? LB + l32 * 16 + w * 2048 : 0x40000u;
#pragma unroll
          for (int j = 0; j < 4; ++j) *(LAS u32x4*)(size_t)(fa + j * 512) = o[j];
          *(LAS u32x2*)(size_t)(own ? LB + 8192 + (w * 32 + l32) * 8 : 0x40000u) = o2;
        }
      }
    }
    STAMP(9);
    WAIT_VM(0);
    STAMP(10);
  }
  {
    int tid = threadIdx.x; asm volatile("" : "+v"(tid));
    const int lane = tid & 63;
    const int tk = tid & 7, cg = (tid >> 3) & 3;
    const int ca = 128 * ((tid >> 5) & 3) + 16 * (tid >> 7) + 4 * cg, cr = 512 + 16 * ((tid >> 5) & 3) + 4 * cg;
    const size_t row = (size_t)(t * 64 + g * 8 + tk) * DM;
    const u32x2 xa = *(const u32x2*)(x + row + ca), xb = *(const u32x2*)(x + row + ca + 64);
    const u32x2 ba = *(const u32x2*)(b2 + ca), bb = *(const u32x2*)(b2 + ca + 64);
    u32x2 xc = xa, bc = ba;
    if (wid < 2) { xc = *(const u32x2*)(x + row + cr); bc = *(const u32x2*)(b2 + cr); }
    WAIT_LGKM(0);
    BAR();
    if (tid == 0) st_flag((unsigned*)(ws + WS_FLAG3) + t * 32 + g, tag);
    const u32x4 own = *(LAS u32x4*)(size_t)(LB + tid * 16);
    u32x2 ownr = {0u, 0u};
    if (wid < 2) ownr = *(LAS u32x2*)(size_t)(LB + 8192 + tid * 8);
    const unsigned sb = WS_SLAB + (t * 8 + g) * 8 * 9216;
    {
      const unsigned* f3 = (const unsigned*)(ws + WS_FLAG3) + t * 32;
      const bool poll = lane < 8 && lane != g;
      unsigned f0 = poll ? ld_flag(f3 + lane) : tag;
#pragma unroll 1
      for (unsigned spins = 0; spins < SPIN_CAP; ++spins) {
        if (__all(f0 == tag)) break;
        __builtin_amdgcn_s_sleep(1);
        if (poll) f0 = ld_flag(f3 + lane);
      }
      asm volatile("" ::: "memory");
    }
    STAMP(11);
    u32x4 v[7];
    u32x2 r[7];
#pragma unroll
    for (int s = 0; s < 7; ++s) {
      const unsigned so = sb + ((g + 1 + s) & 7) * 9216;
      v[s] = __builtin_amdgcn_raw_buffer_load_b128(wsr, so + tid * 16, 0, AUX_SC1);
      if (wid < 2) r[s] = __builtin_amdgcn_raw_buffer_load_b64(wsr, so + 8192 + tid * 8, 0, AUX_SC1);
    }
    auto up = [](u32x2 p) { return (f32x4){bf_lo(p[0]), bf_hi(p[0]), bf_lo(p[1]), bf_hi(p[1])}; };
    f32x4 lo = unpk_lo(own) + (up(xa) + up(ba)), hi = unpk_hi(own) + (up(xb) + up(bb));
#pragma unroll
    for (int s = 0; s < 7; ++s) { lo += unpk_lo(v[s]); hi += unpk_hi(v[s]); }
    *(u32x2*)(out + row + ca) = (u32x2){pk2(lo[0], lo[1]), pk2(lo[2], lo[3])};
    *(u32x2*)(out + row + ca + 64) = (u32x2){pk2(hi[0], hi[1]), pk2(hi[2], hi[3])};
    if (wid < 2) {
      f32x4 m = up(ownr) + (up(xc) + up(bc));
#pragma unroll
      for (int s = 0; s < 7; ++s) m += up(r[s]);
      *(u32x2*)(out + row + cr) = (u32x2){pk2(m[0], m[1]), pk2(m[2], m[3])};
    }
    STAMP(14); STAMP_RT(21);
    STAMP_FLUSH();
    if (blockIdx.x == 0 && tid == 0) st_flag((unsigned*)(ws + WS_EPOCH), tag);
  }
}

extern "C" void kernel_launch(void* const* d_in, const int* in_sizes, int n_in, void* d_out, int out_size, void* d_ws,
                              size_t ws_size, hipStream_t stream) {
  (void)in_sizes; (void)n_in; (void)out_size; (void)ws_size;
  (void)hipFuncSetAttribute((const void*)fused_block_kernel, hipFuncAttributeMaxDynamicSharedMemorySize, LDS_BYTES);
  fused_block_kernel<<<256, 512, LDS_BYTES, stream>>>(
      (const unsigned short*)d_in[0], (const char*)d_in[1], (const char*)d_in[2], (const char*)d_in[4],
      (const unsigned short*)d_in[5], (const char*)d_in[6], (const unsigned short*)d_in[7], (unsigned short*)d_out, (char*)d_ws);
}
```

```cpp
#include <hip/hip_runtime.h>
#include <hip/hip_bf16.h>
#include <type_traits>

typedef __attribute__((ext_vector_type(4))) short s16x4;
typedef __attribute__((ext_vector_type(8))) __bf16 bf16x8;
typedef __attribute__((ext_vector_type(2))) __bf16 bf16x2;
typedef __attribute__((ext_vector_type(4))) float f32x4;
typedef __attribute__((ext_vector_type(2))) float f32x2;
typedef __attribute__((ext_vector_type(4))) unsigned u32x4;
typedef __attribute__((ext_vector_type(2))) unsigned u32x2;
#define LAS __attribute__((address_space(3)))
#define GAS __attribute__((address_space(1)))
#define DI __device__ __forceinline__
#define SCHED() __builtin_amdgcn_sched_barrier(0)
#define BAR() __builtin_amdgcn_s_barrier()
#define WAIT_VM(n) asm volatile("s_waitcnt vmcnt(%0)" ::"n"(n) : "memory")
#define WAIT_LGKM(n) asm volatile("s_waitcnt lgkmcnt(%0)" ::"n"(n) : "memory")

#define STAMP(i) do {} while (0)
#define STAMP_RT(i) do {} while (0)
#define STAMP_DECL
#define STAMP_FLUSH() do {} while (0)

#define CODE_PF 1
#define CODE_PF_BYTES 49152
#define XPOL ""
#define MAP_NGX 2

namespace {

constexpr int DM = 576;
constexpr int ACT_STRIDE = 1184;
constexpr int RING = 64 * ACT_STRIDE;
constexpr int KSLOT = 36864;
constexpr int PQ = RING + 2 * KSLOT;
constexpr int PQ_STRIDE = 144;
constexpr int SIMG = PQ + 64 * PQ_STRIDE;
constexpr int LDS_BYTES = 163840;
constexpr int KTMP = RING + 16384;
constexpr int VTMP = KTMP + 64 * 144;
static_assert(SIMG + 5120 <= LDS_BYTES, "lds");
static_assert(VTMP + 64 * 152 + 16 <= RING + KSLOT, "lds");

constexpr unsigned WS_EPOCH = 0;
constexpr unsigned WS_FLAG1 = 1024;
constexpr unsigned WS_FLAG2 = 2048;
constexpr unsigned WS_FLAG3 = 4096;
constexpr unsigned WS_P = 65536;
constexpr unsigned WS_S = WS_P + 256 * 16384;
constexpr unsigned WS_SLAB = WS_S + 8 * 8192;
constexpr unsigned WS_END = WS_SLAB + 32 * 8 * 8 * 9216;
constexpr unsigned OOB = 0x7ffffff0u;
constexpr int AUX_SC1 = 16;
constexpr unsigned SPIN_CAP = 1u << 16;

DI float bf_lo(unsigned u) { return __builtin_bit_cast(float, u << 16); }
DI float bf_hi(unsigned u) { return __builtin_bit_cast(float, u & 0xffff0000u); }
DI unsigned pk2(float a, float b) {
  f32x2 p = {a, b};
  return __builtin_bit_cast(unsigned, __builtin_convertvector(p, bf16x2));
}
DI float rbf(float a) { return bf_lo(pk2(a, a)); }
template <int CTRL> DI float dpp_add(float v) {
  return v + __builtin_bit_cast(float, __builtin_amdgcn_update_dpp(0, __builtin_bit_cast(int, v), CTRL, 0xf, 0xf, true));
}
DI int fresh_lane() { int v = threadIdx.x & 63; asm volatile("" : "+v"(v)); return v; }
DI bf16x8 lds_b128(unsigned addr) { return *(LAS bf16x8*)(size_t)addr; }
DI s16x4 lds_tr(unsigned addr) { return __builtin_amdgcn_ds_read_tr16_b64_v4i16((LAS s16x4*)(size_t)addr); }
DI bf16x8 join(s16x4 lo, s16x4 hi) {
  return __builtin_bit_cast(bf16x8, __builtin_shufflevector(lo, hi, 0, 1, 2, 3, 4, 5, 6, 7));
}
DI f32x4 mfma(bf16x8 a, bf16x8 b, f32x4 c) { return __builtin_amdgcn_mfma_f32_16x16x32_bf16(a, b, c, 0, 0, 0); }
DI float pos(float v) { const int b = __builtin_bit_cast(int, v); return __builtin_bit_cast(float, b > 0 ? b : 0); }
DI float neg(float v) { const int b = __builtin_bit_cast(int, v); return __builtin_bit_cast(float, b < 0 ? b : 0); }
DI f32x2 relu2(float a, float b) { const f32x2 p = {pos(a), pos(b)}; return p * p; }
DI unsigned phi2(float a, float b) {
  const f32x2 m = (f32x2){a, b} * 1.44269504088896f;
  const f32x2 e = {__builtin_amdgcn_exp2f(neg(m[0])), __builtin_amdgcn_exp2f(neg(m[1]))};
  const f32x2 r = ((f32x2){pos(a), pos(b)} + 1e-6f) + e;
  return __builtin_bit_cast(unsigned, __builtin_convertvector(r, bf16x2));
}
DI unsigned pk2v(f32x2 p) { return __builtin_bit_cast(unsigned, __builtin_convertvector(p, bf16x2)); }
DI unsigned ld_flag(const unsigned* p) { return __hip_atomic_load((GAS unsigned*)p, __ATOMIC_RELAXED, __HIP_MEMORY_SCOPE_AGENT); }
DI void st_flag(unsigned* p, unsigned v) { __hip_atomic_store((GAS unsigned*)p, v, __ATOMIC_RELAXED, __HIP_MEMORY_SCOPE_AGENT); }
DI void wait_flags(const unsigned* base, int count, unsigned tag, int lane, unsigned first) {
  unsigned v = first;
#pragma unroll 1
  for (unsigned spins = 0; spins < SPIN_CAP; ++spins) {
    if (__all(lane >= count || v == tag)) break;
    __builtin_amdgcn_s_sleep(2);
    if (lane < count) v = ld_flag(base + lane);
  }
  asm volatile("" ::: "memory");
}
DI f32x4 unpk_lo(u32x4 v) { return (f32x4){bf_lo(v[0]), bf_hi(v[0]), bf_lo(v[1]), bf_hi(v[1])}; }
DI f32x4 unpk_hi(u32x4 v) { return (f32x4){bf_lo(v[2]), bf_hi(v[2]), bf_lo(v[3]), bf_hi(v[3])}; }

DI void dma(unsigned voff, const char* sbase, unsigned m0v) {
  asm volatile("s_mov_b32 m0, %1\n\ts_nop 0\n\tglobal_load_lds_dwordx4 %0, %2" ::"v"(voff), "s"(m0v), "s"(sbase) : "memory");
}
template <int B, int P> DI void piece(const unsigned (&voff)[5], const char* sbase, unsigned m) {
  asm volatile("s_add_i32 m0, %1, %3\n\ts_nop 0\n\tglobal_load_lds_dwordx4 %0, %2 offset:%4"
               ::"v"(voff[P]), "s"(m), "s"(sbase), "n"(B * 4096 + P * 1024 - B * 128), "n"(B * 128) : "memory", "scc");
}
DI void touch(const char* sbase, unsigned voff, unsigned& dummy) { asm volatile("global_load_dword %0, %1, %2" : "+v"(dummy) : "v"(voff), "s"(sbase) : "memory"); }

template <int BOFF0>
DI void kstep9(f32x4 (&acc)[9][4], bf16x8 (&A)[9], const bf16x8 (&Bc)[4], bf16x8 (&Bn)[4], unsigned ab, unsigned abn, unsigned bn) {
  s16x4 lx, ly;
  constexpr int BTS = 16 * ACT_STRIDE;
#define MM(blk, j) acc[blk][j] = mfma(A[blk], Bc[j], acc[blk][j])
#define RL(v, base, blk) v = lds_tr((base) + (blk) * 4096)
#define RH(v, base, blk) A[blk] = join(v, lds_tr((base) + (blk) * 4096 + 512))
  RL(lx, ab, 3); SCHED(); MM(0, 0); SCHED(); RH(lx, ab, 3); SCHED(); MM(0, 1); SCHED(); MM(0, 2); MM(0, 3); SCHED();
  MM(1, 0); SCHED(); Bn[0] = lds_b128(bn + BOFF0); SCHED(); MM(1, 1); SCHED();
  Bn[1] = lds_b128(bn + BOFF0 + BTS); SCHED(); MM(1, 2); MM(1, 3); SCHED();
  WAIT_LGKM(2);
  BAR();
  RL(lx, ab, 4); SCHED(); MM(2, 0); SCHED(); RH(lx, ab, 4); SCHED(); MM(2, 1); SCHED();
  RL(ly, ab, 5); SCHED(); MM(2, 2); SCHED(); RH(ly, ab, 5); SCHED(); MM(2, 3); SCHED();
  RL(lx, ab, 6); SCHED(); MM(3, 0); SCHED(); RH(lx, ab, 6); SCHED(); MM(3, 1); SCHED(); MM(3, 2); MM(3, 3); SCHED();
  RL(lx, ab, 7); SCHED(); MM(4, 0); SCHED(); RH(lx, ab, 7); SCHED(); MM(4, 1); SCHED(); MM(4, 2); MM(4, 3); SCHED();
  RL(lx, ab, 8); SCHED(); MM(5, 0); SCHED(); RH(lx, ab, 8); SCHED(); MM(5, 1); SCHED(); MM(5, 2); MM(5, 3); SCHED();
  MM(6, 0); SCHED(); Bn[2] = lds_b128(bn + BOFF0 + 2 * BTS); SCHED(); MM(6, 1); SCHED();
  Bn[3] = lds_b128(bn + BOFF0 + 3 * BTS); SCHED(); MM(6, 2); MM(6, 3); SCHED();
  WAIT_LGKM(2);
  BAR();
  RL(lx, abn, 0); SCHED(); MM(7, 0); SCHED(); RH(lx, abn, 0); SCHED(); MM(7, 1); SCHED();
  RL(ly, abn, 1); SCHED(); MM(7, 2); SCHED(); RH(ly, abn, 1); SCHED(); MM(7, 3); SCHED();
  RL(lx, abn, 2); SCHED(); MM(8, 0); SCHED(); RH(lx, abn, 2); SCHED(); MM(8, 1); SCHED(); MM(8, 2); MM(8, 3); SCHED();
}
template <int BOFF0>
DI void kstep4(f32x4 (&acc)[4][4], bf16x8 (&A)[4], const bf16x8 (&Bc)[4], bf16x8 (&Bn)[4], unsigned ab, unsigned abn, unsigned d3, unsigned bn) {
  s16x4 lx, ly;
  constexpr int BTS = 16 * ACT_STRIDE;
#define QL(v, base, off) v = lds_tr((base) + (off))
#define QH(v, i, base, off) A[i] = join(v, lds_tr((base) + (off) + 576))
  const unsigned a3 = ab + d3;
  QL(lx, a3, 0); SCHED(); MM(0, 0); SCHED(); QH(lx, 3, a3, 0); SCHED(); MM(0, 1); SCHED();
  Bn[0] = lds_b128(bn + BOFF0); SCHED(); MM(0, 2); SCHED(); Bn[1] = lds_b128(bn + BOFF0 + BTS); SCHED(); MM(0, 3); SCHED();
  Bn[2] = lds_b128(bn + BOFF0 + 2 * BTS); SCHED(); MM(1, 0); SCHED(); Bn[3] = lds_b128(bn + BOFF0 + 3 * BTS); SCHED();
  MM(1, 1); MM(1, 2); MM(1, 3); SCHED();
  WAIT_LGKM(4);
  BAR();
  QL(lx, abn, 0); SCHED(); MM(2, 0); SCHED(); QH(lx, 0, abn, 0); SCHED(); MM(2, 1); SCHED();
  QL(ly, abn, 5120); SCHED(); MM(2, 2); SCHED(); QH(ly, 1, abn, 5120); SCHED(); MM(2, 3); SCHED();
  QL(lx, abn, 10240); SCHED(); MM(3, 0); SCHED(); QH(lx, 2, abn, 10240); SCHED(); MM(3, 1); MM(3, 2); MM(3, 3); SCHED();
}
#undef MM

}

__global__ __launch_bounds__(512, 2) void fused_block_kernel(
    const unsigned short* __restrict__ x, const char* __restrict__ wqkv, const char* __restrict__ wo,
    const char* __restrict__ w1, const unsigned short* __restrict__ b1, const char* __restrict__ w2,
    const unsigned short* __restrict__ b2, unsigned short* __restrict__ out, char* __restrict__ ws) {
  extern __shared__ __attribute__((aligned(16))) char lds[];
  const int tid = threadIdx.x, lane = tid & 63;
  const int wid = __builtin_amdgcn_readfirstlane(tid >> 6);
  constexpr int NGX = MAP_NGX, PFN = 32 / NGX;
  const int xj = blockIdx.x & 7, xi = blockIdx.x >> 3;
  const int g = NGX * (xj / NGX) + xi % NGX, pfr = xi / NGX, t = (xj % NGX) * PFN + pfr;
  const unsigned LB = (unsigned)(size_t)lds;
  const __amdgpu_buffer_rsrc_t wsr = __builtin_amdgcn_make_buffer_rsrc(ws, 0, WS_END, 0x00020000);

  unsigned long long pc0;
  asm volatile("s_getpc_b64 %0" : "=s"(pc0));
  STAMP_DECL
  STAMP(0); STAMP_RT(20);
  const int lr = lane >> 3, lc = lane & 7;
  const int l = wid - 4;
  const int cE = lc ^ (((lr >> 1) & 1) << 1), cO = cE ^ 4;
  unsigned voff[5];
  const unsigned m0r = LB + RING;
  const char* baseQ = wqkv + g * 144 + l * 1152;
  auto issueQ = [&](const char* sq, unsigned mq) {
    dma(voff[0], sq, mq); dma(voff[1], sq, mq + 1024); dma(voff[2], sq, mq + 2048); dma(voff[3], sq, mq + 3072); dma(voff[4], sq, mq + 4096);
  };
  unsigned dummy = 0;
  unsigned epoch;
  if (wid < 4) {
    u32x4 xr[2][9];
    const char* xp = (const char*)x + (size_t)(t * 64 + wid * 16) * 1152;
    const unsigned xo = lr * 1152 + lc * 16;
#pragma unroll
    for (int h = 0; h < 2; ++h)
#pragma unroll
      for (int i = 0; i < 9; ++i)
        asm volatile("global_load_dwordx4 %0, %1, %2 offset:%3 " XPOL : "=v"(xr[h][i]) : "v"(xo + h * 8 * 1152), "s"(xp), "n"(i * 128) : "memory");
    asm volatile("global_load_dword %0, %1, %2 sc1" : "=v"(epoch) : "v"(0u), "s"(ws) : "memory");
#pragma unroll
    for (int h = 0; h < 2; ++h) {
      if (h == 0) WAIT_VM(10); else WAIT_VM(0);
      SCHED();
#pragma unroll
      for (int i = 0; i < 9; ++i) asm volatile("" : "+v"(xr[h][i]));
      if (h == 0) STAMP(1);
      if (h == 1) {
        constexpr int RW1 = 576 / PFN, NP1 = (RW1 * 9 + 255) / 256;
        {
          const char* p1 = w1 + (size_t)(RW1 * pfr) * 9216 + g * 1152;
#pragma unroll
          for (int n = 0; n < NP1; ++n) {
            const int i0 = (4 * n + wid) * 64 + lane, i = i0 < RW1 * 9 ? i0 : 0;
            const int r = (i * 7282) >> 16;
            touch(p1, r * 9216 + (i - 9 * r) * 128, dummy);
          }
        }
      }
      const bf16x2 ones = __builtin_bit_cast(bf16x2, 0x3f803f80u);
      float s0 = 0.f, s1 = 0.f, q0 = 0.f, q1 = 0.f;
#pragma unroll
      for (int i = 0; i < 9; ++i) {
        const unsigned u0 = xr[h][i][0], u1 = xr[h][i][1], u2 = xr[h][i][2], u3 = xr[h][i][3];
        const bf16x2 a = __builtin_bit_cast(bf16x2, u0), b = __builtin_bit_cast(bf16x2, u1);
        const bf16x2 c = __builtin_bit_cast(bf16x2, u2), d = __builtin_bit_cast(bf16x2, u3);
        s0 = __builtin_amdgcn_fdot2_f32_bf16(a, ones, s0, false); s1 = __builtin_amdgcn_fdot2_f32_bf16(b, ones, s1, false);
        q0 = __builtin_amdgcn_fdot2_f32_bf16(a, a, q0, false); q1 = __builtin_amdgcn_fdot2_f32_bf16(b, b, q1, false);
        s0 = __builtin_amdgcn_fdot2_f32_bf16(c, ones, s0, false); s1 = __builtin_amdgcn_fdot2_f32_bf16(d, ones, s1, false);
        q0 = __builtin_amdgcn_fdot2_f32_bf16(c, c, q0, false); q1 = __builtin_amdgcn_fdot2_f32_bf16(d, d, q1, false);
      }
      float s = s0 + s1, q = q0 + q1;
      s = dpp_add<0xB1>(s); q = dpp_add<0xB1>(q);
      s = dpp_add<0x4E>(s); q = dpp_add<0x4E>(q);
      s = dpp_add<0x141>(s); q = dpp_add<0x141>(q);
      const float mean = s * (1.f / 576.f);
      const float var = fmaxf(q * (1.f / 576.f) - mean * mean, 0.f);
      const float mb = rbf(mean);
      const float sd = rbf(__builtin_amdgcn_sqrtf(rbf(rbf(var) + 1e-5f)));
      const float rs = __builtin_amdgcn_rcpf(sd), nb = -mb * rs;
      const unsigned wa = LB + (wid * 16 + h * 8 + lr) * ACT_STRIDE + lc * 16;
#pragma unroll
      for (int i = 0; i < 9; ++i) {
        u32x4 o;
#pragma unroll
        for (int e = 0; e < 4; ++e) o[e] = pk2(fmaf(bf_lo(xr[h][i][e]), rs, nb), fmaf(bf_hi(xr[h][i][e]), rs, nb));
        *(LAS u32x4*)(size_t)(wa + i * 128) = o;
      }
    }
  } else {
    if (wid == 7) {
      const unsigned i = (unsigned)lane * 32u + (unsigned)xi;
      touch((const char*)pc0, (i < CODE_PF_BYTES / 128 ? i : 0u) * 128u, dummy);
    }
    asm volatile("global_load_dword %0, %1, %2 sc1" : "=v"(epoch) : "v"(0u), "s"(ws) : "memory");
    if (wid == 7) {
      constexpr int RW = 576 / PFN, NQ = (RW + 7) / 8;
      const char* pq_ = wqkv + (size_t)(RW * pfr) * 3456 + ((g * 144) & ~127);
      const int c6 = lc < 5 ? lc : 5;
#pragma unroll
      for (int n = 0; n < NQ; ++n) {
        const int r = 8 * n + lr < RW ? 8 * n + lr : RW - 1;
        touch(pq_, r * 3456 + (c6 >> 1) * 1152 + (c6 & 1) * 128, dummy);
      }
      WAIT_VM(NQ);
    } else {
#pragma unroll
      for (int p = 0; p < 5; ++p) {
        int i = 64 * p + lane; i = i < 288 ? i : 287;
        const int r = (i * 7282) >> 16;
        voff[p] = r * 3456 + (i - 9 * r) * 16;
      }
      issueQ(baseQ, m0r + l * 5120);
      WAIT_VM(5);
    }
    SCHED();
    STAMP(1);
  }
  asm volatile("" : "+v"(epoch));
  const unsigned tag = __builtin_amdgcn_readfirstlane(epoch) + 1u;
  STAMP(2);

  if (wid >= 4) {
#define LPRIO 2
#define CPRIO 3
    __builtin_amdgcn_s_setprio(LPRIO);
    const char* base1 = w1 + g * 1152;
    const char* base2 = w2 + (size_t)(g * 576) * 1152;
    const char* baseO = wo + (size_t)(g * 72) * 1152;
    auto setvoff = [&](unsigned ldw, int rowsel) {
#pragma unroll
      for (int p = 0; p < 4; ++p) voff[p] = (rowsel * 8 * p + lr) * ldw + ((p & 1) ? cO : cE) * 16;
    };
    using I1 = std::integral_constant<int, 1>;
    WAIT_LGKM(0);
    if (l < 3) {
      auto prog = [&](auto LC) {
        constexpr int L = decltype(LC)::value;
        auto issue8 = [&](const char* sb, unsigned m) {
          if constexpr (L == 0) { piece<8, 0>(voff, sb, m); piece<8, 3>(voff, sb, m); }
          if constexpr (L == 1) piece<8, 1>(voff, sb, m);
          if constexpr (L == 2) piece<8, 2>(voff, sb, m);
        };
        auto issueLo = [&](const char* sb, unsigned m) {
          if constexpr (L == 0) { piece<0, 2>(voff, sb, m); piece<1, 1>(voff, sb, m); piece<2, 0>(voff, sb, m); piece<2, 3>(voff, sb, m); piece<3, 2>(voff, sb, m); }
          if constexpr (L == 1) { piece<0, 0>(voff, sb, m); piece<0, 3>(voff, sb, m); piece<1, 2>(voff, sb, m); piece<2, 1>(voff, sb, m); piece<3, 0>(voff, sb, m); piece<3, 3>(voff, sb, m); }
          if constexpr (L == 2) { piece<0, 1>(voff, sb, m); piece<1, 0>(voff, sb, m); piece<1, 3>(voff, sb, m); piece<2, 2>(voff, sb, m); piece<3, 1>(voff, sb, m); }
        };
        auto issueHi = [&](const char* sb, unsigned m) {
          if constexpr (L == 0) { piece<4, 1>(voff, sb, m); piece<5, 0>(voff, sb, m); piece<5, 3>(voff, sb, m); piece<6, 2>(voff, sb, m); piece<7, 1>(voff, sb, m); }
          if constexpr (L == 1) { piece<4, 2>(voff, sb, m); piece<5, 1>(voff, sb, m); piece<6, 0>(voff, sb, m); piece<6, 3>(voff, sb, m); piece<7, 2>(voff, sb, m); }
          if constexpr (L == 2) { piece<4, 0>(voff, sb, m); piece<4, 3>(voff, sb, m); piece<5, 2>(voff, sb, m); piece<6, 1>(voff, sb, m); piece<7, 0>(voff, sb, m); piece<7, 3>(voff, sb, m); }
        };
        constexpr int WX = L == 0 ? 10 : 11;
        const unsigned mA = m0r, mB = m0r + KSLOT;
        {
          const char* sq = baseQ + 32 * 3456;
          const unsigned mq = m0r + L * 5120;
          issueQ(sq, mq + 16384); issueQ(sq + 32 * 3456, mq + KSLOT);
          sq += 64 * 3456;
          STAMP(15);
          WAIT_VM(10);
          STAMP(16);
          BAR();
          STAMP(3);
          issueQ(sq, mq + KSLOT + 16384); WAIT_VM(10); BAR();
          sq += 32 * 3456;
#pragma unroll 1
          for (int j = 0; j < 3; ++j) {
            issueQ(sq, mq); WAIT_VM(10); BAR();
            issueQ(sq + 32 * 3456, mq + 16384); WAIT_VM(10); BAR();
            issueQ(sq + 64 * 3456, mq + KSLOT); WAIT_VM(10); BAR();
            issueQ(sq + 96 * 3456, mq + KSLOT + 16384); WAIT_VM(10); BAR();
            sq += 128 * 3456;
          }
          issueQ(sq, mq); WAIT_VM(10); BAR();
          issueQ(sq + 32 * 3456, mq + 16384); WAIT_VM(10); BAR();
        }
        setvoff(9216, 1);
        const char* sb = base1;
        issueLo(sb, mB); WAIT_VM(10); BAR();
        issueHi(sb, mB); issue8(sb, mB); WAIT_VM(12); BAR();
        issueLo(sb + 32 * 9216, mA); BAR(); BAR();
        WAIT_VM(12); BAR();
        auto kstepL = [&](const char* s1, unsigned m1, const char* s2, unsigned m2, auto mid) {
          issueHi(s1, m1); mid(); WAIT_VM(WX); BAR();
          issue8(s1, m1); issueLo(s2, m2); WAIT_VM(12); BAR();
        };
        auto nop = []() {};
        sb = base1 + 32 * 9216;
#pragma unroll 1
        for (int it = 0; it < 8; ++it) {
          kstepL(sb, mA, sb + 32 * 9216, mB, nop);
          kstepL(sb + 32 * 9216, mB, sb + 64 * 9216, mA, nop);
          sb += 64 * 9216;
        }
        issueHi(sb, mA); WAIT_VM(WX); BAR();
        issue8(sb, mA); setvoff(1152, 1); issueLo(base2, mB); WAIT_VM(12); BAR();
        sb = base2;
        kstepL(sb, mB, sb + 32 * 1152, mA, nop);
        kstepL(sb + 32 * 1152, mA, sb + 64 * 1152, mB, [&]() { BAR(); });
        sb += 64 * 1152;
#pragma unroll 1
        for (int it = 0; it < 7; ++it) {
          kstepL(sb, mB, sb + 32 * 1152, mA, nop);
          kstepL(sb + 32 * 1152, mA, sb + 64 * 1152, mB, nop);
          sb += 64 * 1152;
        }
        kstepL(sb, mB, sb + 32 * 1152, mA, nop);
        sb -= 128 * 1152;
        kstepL(sb + 160 * 1152, mA, baseO, mB, nop);
        kstepL(baseO, mB, baseO + 32 * 1152, mA, nop);
        issueHi(baseO + 32 * 1152, mA); WAIT_VM(WX); BAR();
        issue8(baseO + 32 * 1152, mA); setvoff(1152, 0); issueLo(baseO + 64 * 1152, mB); WAIT_VM(12); BAR();
        issueHi(baseO + 64 * 1152, mB); WAIT_VM(WX); BAR();
        issue8(baseO + 64 * 1152, mB); WAIT_VM(6); BAR();
        WAIT_VM(0); BAR(); BAR();
      };
      if (l == 0) prog(std::integral_constant<int, 0>{});
      else if (l == 1) prog(std::integral_constant<int, 1>{});
      else prog(std::integral_constant<int, 2>{});
    } else {
      auto bars = [&](int n) {
#pragma unroll 1
        for (int i = 0; i < n; ++i) BAR();
      };
      const int lane = fresh_lane();
      constexpr int RW = 576 / PFN;
      BAR();
      STAMP(3);
      bars(17 + 1);
      {
        const char* p2 = base2 + (size_t)(RW * pfr) * 1152;
#pragma unroll
        for (int n = 0; n < (RW * 9 + 63) / 64; ++n) {
          const int i = lane + 64 * n < RW * 9 ? lane + 64 * n : 0;
          touch(p2, i * 128, dummy);
        }
        constexpr int WOL = (648 + PFN - 1) / PFN;
        static_assert(WOL <= 64, "prefetch steps");
        int i = pfr * WOL + lane; i = (lane < WOL && i < 648) ? i : 0;
        touch(baseO, i * 128, dummy);
      }
      bars(2);
      bars(13);
      if (lane == 0) st_flag((unsigned*)(ws + WS_FLAG1) + g * 32 + t, tag);
      bars(7);
      unsigned fl = lane < 32 ? ld_flag((const unsigned*)(ws + WS_FLAG1) + g * 32 + lane) : tag;
      bars(3);
      STAMP(16);
      wait_flags((const unsigned*)(ws + WS_FLAG1) + g * 32, 32, tag, lane, fl);
      STAMP(17);
      {
        u32x4 hv[16];
        const unsigned po = WS_P + (g * 32 + (lane >> 5)) * 16384 + t * 512 + (lane & 31) * 16;
#pragma unroll
        for (int n = 0; n < 16; ++n)
          hv[n] = __builtin_amdgcn_raw_buffer_load_b128(wsr, po, n * 32768, AUX_SC1);
        SCHED();
        bars(5);
        f32x4 a = __builtin_bit_cast(f32x4, hv[0]), b = __builtin_bit_cast(f32x4, hv[1]);
#pragma unroll
        for (int n = 2; n < 16; n += 2) { a += __builtin_bit_cast(f32x4, hv[n]); b += __builtin_bit_cast(f32x4, hv[n + 1]); }
        a += b;
        SCHED();
        BAR();
        const int hh = lane >> 5, li = 32 * (t & 1) + (lane & 31), tile = t >> 1;
        float s0 = hh ? a[2] : a[0], s1 = hh ? a[3] : a[1], o0 = hh ? a[0] : a[2], o1 = hh ? a[1] : a[3];
        const unsigned x0 = (unsigned)__builtin_amdgcn_permlane32_swap(__builtin_bit_cast(unsigned, o0), __builtin_bit_cast(unsigned, o0), false, false)[hh ? 0 : 1];
        const unsigned x1 = (unsigned)__builtin_amdgcn_permlane32_swap(__builtin_bit_cast(unsigned, o1), __builtin_bit_cast(unsigned, o1), false, false)[hh ? 0 : 1];
        s0 += __builtin_bit_cast(float, x0); s1 += __builtin_bit_cast(float, x1);
        const int di = (int)((0x4433322221111000ull >> (4 * tile)) & 15u), ej = (int)((0x4343243214210410ull >> (4 * tile)) & 15u);
        const int d = 16 * di + 4 * (li >> 4) + 2 * hh, e = 16 * ej + (li & 15);
        const int hd = (d * 57) >> 10, he = (e * 57) >> 10;
        const bool ok = d < 72 && (e == 72 || (e < 72 && hd == he));
        const int row = e == 72 ? 18 : e - 18 * he;
        __builtin_amdgcn_raw_buffer_store_b32(pk2(s0, s1), wsr, ok ? WS_S + g * 8192 + hd * 1280 + row * 64 + (d - 18 * hd) * 2 : OOB, 0, AUX_SC1);
      }
      bars(5);
      WAIT_VM(0);
      if (lane == 0) st_flag((unsigned*)(ws + WS_FLAG2) + g * 32 + t, tag);
      bars(2 + 1 + 7);
      fl = lane < 32 ? ld_flag((const unsigned*)(ws + WS_FLAG2) + g * 32 + lane) : tag;
      bars(3);
      STAMP(18);
      wait_flags((const unsigned*)(ws + WS_FLAG2) + g * 32, 32, tag, lane, fl);
      STAMP(19);
      {
        u32x4 iv[5];
#pragma unroll
        for (int n = 0; n < 5; ++n) iv[n] = __builtin_amdgcn_raw_buffer_load_b128(wsr, WS_S + g * 8192 + lane * 16, n * 1024, AUX_SC1);
        SCHED();
        bars(5);
        const int c = lane & 3;
#pragma unroll
        for (int n = 0; n < 5; ++n) {
          const int row = 16 * n + (lane >> 2);
          const bool live = row != 19 && row != 39 && row != 59 && row != 79 && c < 3;
          u32x4 v = iv[n];
          v[0] = live ? v[0] : 0u;
          v[1] = (live && c < 2) ? v[1] : 0u; v[2] = (live && c < 2) ? v[2] : 0u; v[3] = (live && c < 2) ? v[3] : 0u;
          *(LAS u32x4*)(size_t)(LB + SIMG + n * 1024 + lane * 16) = v;
        }
        WAIT_LGKM(0);
      }
      bars(27);
    }
    STAMP(8);
    WAIT_VM(0);
    asm volatile("" ::"v"(dummy));
  } else {
    const int w = wid;
    __builtin_amdgcn_s_setprio(CPRIO);
    const int g4 = lane >> 4, q = (lane & 15) >> 2, p = lane & 3;
    const int cf = (((q >> 1) & 1) << 1) | ((g4 & 1) << 2);
    unsigned a0 = LB + RING + (8 * g4 + q) * 128 + (((2 * w + (p >> 1)) ^ cf) * 16) + (p & 1) * 8;
    unsigned a1 = a0 + KSLOT;
    asm volatile("" : "+v"(a1));
    const unsigned b0 = LB + (lane & 15) * ACT_STRIDE + g4 * 16;
    bf16x8 B0[4], B1[4];
    f32x4 acc[9][4];
    {
      f32x4 qa[4][4];
      bf16x8 A[4];
#pragma unroll
      for (int i = 0; i < 4; ++i)
#pragma unroll
        for (int j = 0; j < 4; ++j) qa[i][j] = (f32x4){0.f, 0.f, 0.f, 0.f};
      STAMP(15);
      WAIT_LGKM(0);
      BAR();
      STAMP(3);
#pragma unroll
      for (int j = 0; j < 4; ++j) B0[j] = lds_b128(b0 + j * 16 * ACT_STRIDE);
      unsigned ac = LB + RING + (8 * g4 + q) * 144 + 32 * w + 8 * p, an = ac + KSLOT, bc = b0;
      const unsigned d3 = (w < 3 ? w : 0) * 5120 + 128 - 32 * w;
      A[0] = join(lds_tr(ac), lds_tr(ac + 576));
      A[1] = join(lds_tr(ac + 5120), lds_tr(ac + 5120 + 576));
      A[2] = join(lds_tr(ac + 10240), lds_tr(ac + 10240 + 576));
#pragma unroll 1
      for (int it = 0; it < 9; ++it) {
        kstep4<64>(qa, A, B0, B1, ac, ac + 16384, d3, bc);
        kstep4<128>(qa, A, B1, B0, ac + 16384, an, d3, bc);
        const unsigned tmp = ac; ac = an; an = tmp;
        bc += 128;
      }
      STAMP(4);
      const int lane = fresh_lane(), g4 = lane >> 4, q = (lane & 15) >> 2, p = lane & 3;
      u32x2 bb[9];
#pragma unroll
      for (int blk = 0; blk < 9; ++blk) bb[blk] = *(const u32x2*)(b1 + g * 576 + 64 * blk + 16 * w + 4 * g4);
      {
        const unsigned tk = (lane & 15), col = (16 * w + 4 * g4) * 2;
        const unsigned aq = LB + PQ + tk * 144 + col, ak = LB + KTMP + tk * 144 + col, av = LB + VTMP + tk * 152 + col;
        constexpr unsigned DROP = 0x40000;
        const unsigned ar = w == 3 ? (g4 == 0 ? LB + VTMP + tk * 152 + 144 : DROP)
                                   : g4 >= 2 ? DROP : (w == 0 ? LB + PQ + tk * 144 : w == 1 ? LB + KTMP + tk * 144 : LB + VTMP + tk * 152) + 128 + g4 * 8;
        const unsigned rs = w >= 2 ? 16 * 152 : 16 * 144;
        u32x2 oq[4], ok[4], ov[4], orm[4];
#pragma unroll
        for (int tt = 0; tt < 4; ++tt) {
          ov[tt] = (u32x2){pk2(qa[2][tt][0], qa[2][tt][1]), pk2(qa[2][tt][2], qa[2][tt][3])};
          ok[tt] = (u32x2){phi2(qa[1][tt][0], qa[1][tt][1]), phi2(qa[1][tt][2], qa[1][tt][3])};
        }
        SCHED();
#pragma unroll
        for (int tt = 0; tt < 4; ++tt) {
          *(LAS u32x2*)(size_t)(av + tt * 16 * 152) = ov[tt];
          *(LAS u32x2*)(size_t)(ak + tt * 16 * 144) = ok[tt];
        }
        if (w < 2) {
#pragma unroll
          for (int tt = 0; tt < 4; ++tt) orm[tt] = (u32x2){phi2(qa[3][tt][0], qa[3][tt][1]), phi2(qa[3][tt][2], qa[3][tt][3])};
        } else {
#pragma unroll
          for (int tt = 0; tt < 4; ++tt) {
            orm[tt] = (u32x2){pk2(qa[3][tt][0], qa[3][tt][1]), pk2(qa[3][tt][2], qa[3][tt][3])};
            if (w == 3) orm[tt] = (u32x2){0x00003f80u, 0u};
          }
        }
#pragma unroll
        for (int tt = 0; tt < 4; ++tt) oq[tt] = (u32x2){phi2(qa[0][tt][0], qa[0][tt][1]), phi2(qa[0][tt][2], qa[0][tt][3])};
        SCHED();
#pragma unroll
        for (int tt = 0; tt < 4; ++tt) {
          *(LAS u32x2*)(size_t)(ar + tt * rs) = orm[tt];
          *(LAS u32x2*)(size_t)(aq + tt * 16 * 144) = oq[tt];
        }
      }
      WAIT_LGKM(0);
      BAR();
      const unsigned ti = (unsigned)(0x4433322221111000ull >> (16 * w)) & 0xffffu;
      const unsigned tj = (unsigned)(0x4343243214210410ull >> (16 * w)) & 0xffffu;
      const unsigned kr = LB + KTMP + (8 * g4 + q) * 144 + p * 8, vr = LB + VTMP + (8 * g4 + q) * 152 + p * 8;
#pragma unroll
      for (int n = 0; n < 4; ++n) {
        const unsigned di = (ti >> (4 * n)) & 15u, ej = (tj >> (4 * n)) & 15u;
        f32x4 d = {0.f, 0.f, 0.f, 0.f};
#pragma unroll
        for (int ks = 0; ks < 2; ++ks) {
          const unsigned ka = kr + ks * 32 * 144 + di * 32, va = vr + ks * 32 * 152 + ej * 32;
          const bf16x8 fa = join(lds_tr(ka), lds_tr(ka + 4 * 144));
          const bf16x8 fb = join(lds_tr(va), lds_tr(va + 4 * 152));
          d = mfma(fa, fb, d);
        }
        __builtin_amdgcn_raw_buffer_store_b128(__builtin_bit_cast(u32x4, d), wsr,
                                               WS_P + (g * 32 + t) * 16384 + (4 * w + n) * 1024 + lane * 16, 0, AUX_SC1);
      }
#pragma unroll
      for (int i = 0; i < 9; ++i) {
        const f32x4 bv = {bf_lo(bb[i][0]), bf_hi(bb[i][0]), bf_lo(bb[i][1]), bf_hi(bb[i][1])};
#pragma unroll
        for (int j = 0; j < 4; ++j) acc[i][j] = bv;
      }
      WAIT_LGKM(0);
      BAR();
      STAMP(5);
    }
    bf16x8 A[9];
    {
#pragma unroll
      for (int j = 0; j < 4; ++j) B0[j] = lds_b128(b0 + j * 16 * ACT_STRIDE);
      A[0] = join(lds_tr(a1), lds_tr(a1 + 512));
      A[1] = join(lds_tr(a1 + 4096), lds_tr(a1 + 4096 + 512));
      A[2] = join(lds_tr(a1 + 8192), lds_tr(a1 + 8192 + 512));
      unsigned bc = b0;
#pragma unroll 1
      for (int it = 0; it < 9; ++it) {
        if (__builtin_expect(it == 3, 0)) { WAIT_VM(0); asm volatile("" ::"v"(dummy)); }
        kstep9<64>(acc, A, B0, B1, a1, a0, bc);
        kstep9<128>(acc, A, B1, B0, a0, a1, bc);
        bc += 128;
      }
      STAMP(6);
      const int lane = fresh_lane(), g4 = lane >> 4;
      const unsigned ha = LB + (lane & 15) * ACT_STRIDE + (16 * w + 8 * (g4 >> 1)) * 2;
      u32x4 hq[18];
#pragma unroll
      for (int n = 0; n < 18; ++n) {
        const f32x4 u = n < 16 ? acc[2 * (n & 3)][n >> 2] : acc[8][2 * (n - 16)];
        const f32x4 v = n < 16 ? acc[2 * (n & 3) + 1][n >> 2] : acc[8][2 * (n - 16) + 1];
        const auto s0 = __builtin_amdgcn_permlane16_swap(pk2v(relu2(u[0], u[1])), pk2v(relu2(v[0], v[1])), false, false);
        const auto s1 = __builtin_amdgcn_permlane16_swap(pk2v(relu2(u[2], u[3])), pk2v(relu2(v[2], v[3])), false, false);
        hq[n] = (u32x4){(unsigned)s0[0], (unsigned)s1[0], (unsigned)s0[1], (unsigned)s1[1]};
        if (n >= 2) {
          SCHED();
          const int m = n - 2;
          *(LAS u32x4*)(size_t)(ha + (m >> 2) * 16 * ACT_STRIDE + (128 * (m & 3) + 64 * (g4 & 1)) * 2) = hq[m];
          SCHED();
        }
      }
      *(LAS u32x4*)(size_t)(ha + (g4 & 1) * 16 * ACT_STRIDE + 512 * 2) = hq[16];
      *(LAS u32x4*)(size_t)(ha + (2 + (g4 & 1)) * 16 * ACT_STRIDE + 512 * 2) = hq[17];
      WAIT_LGKM(0);
      BAR();
      STAMP(7);
    }
    {
#pragma unroll
      for (int j = 0; j < 4; ++j) B0[j] = lds_b128(b0 + j * 16 * ACT_STRIDE);
#pragma unroll
      for (int i = 0; i < 9; ++i)
#pragma unroll
        for (int j = 0; j < 4; ++j) acc[i][j] = (f32x4){0.f, 0.f, 0.f, 0.f};
      unsigned bc = b0;
      kstep9<64>(acc, A, B0, B1, a1, a0, bc);
#pragma unroll 1
      for (int it = 0; it < 10; ++it) {
        if (__builtin_expect(it == 6, 0)) {
          const int lane = fresh_lane(), g4 = lane >> 4, r16 = lane & 15;
          const unsigned sa = LB + SIMG + w * 1280 + g4 * 16;
          const bf16x8 ka0 = lds_b128(sa + r16 * 64), ka1 = lds_b128(sa + (r16 < 3 ? 16 + r16 : 19) * 64);
          const unsigned pa = LB + PQ + r16 * PQ_STRIDE + w * 36 + g4 * 16;
          const unsigned oa = LB + r16 * ACT_STRIDE + w * 36 + g4 * 8;
          const unsigned la = g4 == 0 ? oa + 32 : LB + r16 * ACT_STRIDE + 144 + w * 12 + (g4 - 1) * 4;
          u32x4 pb[4];
#pragma unroll
          for (int tt = 0; tt < 4; ++tt)
#pragma unroll
            for (int m = 0; m < 4; ++m) pb[tt][m] = *(LAS unsigned*)(size_t)(pa + tt * 16 * PQ_STRIDE + 4 * m);
#pragma unroll
          for (int tt = 0; tt < 4; ++tt) {
            const bf16x8 b = __builtin_bit_cast(bf16x8, pb[tt]);
            const f32x4 n0 = mfma(ka0, b, (f32x4){0.f, 0.f, 0.f, 0.f}), n1 = mfma(ka1, b, (f32x4){0.f, 0.f, 0.f, 0.f});
            const float d2 = n1[2];
            const unsigned du = __builtin_bit_cast(unsigned, d2);
            const unsigned y = (unsigned)__builtin_amdgcn_permlane32_swap(du, du, false, false)[0];
            const unsigned z = (unsigned)__builtin_amdgcn_permlane16_swap(y, y, false, false)[0];
            const float r = __builtin_amdgcn_rcpf(__builtin_bit_cast(float, z) + 1e-6f);
            const f32x2 p0 = (f32x2){n0[0], n0[1]} * r, p1 = (f32x2){n0[2], n0[3]} * r, p2 = (f32x2){n1[0], n1[1]} * r;
            *(LAS unsigned*)(size_t)(oa + tt * 16 * ACT_STRIDE) = pk2v(p0);
            *(LAS unsigned*)(size_t)(oa + tt * 16 * ACT_STRIDE + 4) = pk2v(p1);
            *(LAS unsigned*)(size_t)(la + tt * 16 * ACT_STRIDE) = g4 == 0 ? pk2v(p2) : 0u;
          }
        }
        kstep9<128>(acc, A, B1, B0, a0, a1, bc);
        kstep9<192>(acc, A, B0, B1, a1, a0, bc);
        bc += it == 7 ? 128 - 18 * 64 : 128;
      }
    }
    STAMP(8);
    {
      const int lane = fresh_lane(), g4 = lane >> 4;
      const int hf = (lane >> 3) & 1, l32 = g4 * 8 + (lane & 7);
#pragma unroll
      for (int tq = 0; tq < 4; ++tq) {
        const int tt = tq;
        const bool own = 2 * tt + hf == g;
        const unsigned sb = own ? OOB : WS_SLAB + ((t * 8 + 2 * tt + hf) * 8 + g) * 9216;
        u32x4 o[4];
#pragma unroll
        for (int j = 0; j < 4; ++j) {
          o[j][0] = pk2(acc[2 * j][tt][0], acc[2 * j][tt][1]); o[j][1] = pk2(acc[2 * j][tt][2], acc[2 * j][tt][3]);
          o[j][2] = pk2(acc[2 * j + 1][tt][0], acc[2 * j + 1][tt][1]); o[j][3] = pk2(acc[2 * j + 1][tt][2], acc[2 * j + 1][tt][3]);
        }
        const u32x2 o2 = {pk2(acc[8][tt][0], acc[8][tt][1]), pk2(acc[8][tt][2], acc[8][tt][3])};
#pragma unroll
        for (int j = 0; j < 4; ++j) __builtin_amdgcn_raw_buffer_store_b128(o[j], wsr, sb + ((w * 4 + j) * 32 + l32) * 16, 0, AUX_SC1);
        __builtin_amdgcn_raw_buffer_store_b64(o2, wsr, sb + 8192 + (w * 32 + l32) * 8, 0, AUX_SC1);
        if (tt == (g >> 1)) {
          const unsigned fa = own ? LB + l32 * 16 + w * 2048 : 0x40000u;
#pragma unroll
          for (int j = 0; j < 4; ++j) *(LAS u32x4*)(size_t)(fa + j * 512) = o[j];
          *(LAS u32x2*)(size_t)(own ? LB + 8192 + (w * 32 + l32) * 8 : 0x40000u) = o2;
        }
      }
    }
    STAMP(9);
    WAIT_VM(0);
    STAMP(10);
  }
  {
    int tid = threadIdx.x; asm volatile("" : "+v"(tid));
    const int lane = tid & 63;
    const int tk = tid & 7, cg = (tid >> 3) & 3;
    const int ca = 128 * ((tid >> 5) & 3) + 16 * (tid >> 7) + 4 * cg, cr = 512 + 16 * ((tid >> 5) & 3) + 4 * cg;
    const size_t row = (size_t)(t * 64 + g * 8 + tk) * DM;
    const u32x2 xa = *(const u32x2*)(x + row + ca), xb = *(const u32x2*)(x + row + ca + 64);
    const u32x2 ba = *(const u32x2*)(b2 + ca), bb = *(const u32x2*)(b2 + ca + 64);
    u32x2 xc = xa, bc = ba;
    if (wid < 2) { xc = *(const u32x2*)(x + row + cr); bc = *(const u32x2*)(b2 + cr); }
    WAIT_LGKM(0);
    BAR();
    if (tid == 0) st_flag((unsigned*)(ws + WS_FLAG3) + t * 32 + g, tag);
    const u32x4 own = *(LAS u32x4*)(size_t)(LB + tid * 16);
    u32x2 ownr = {0u, 0u};
    if (wid < 2) ownr = *(LAS u32x2*)(size_t)(LB + 8192 + tid * 8);
    const unsigned sb = WS_SLAB + (t * 8 + g) * 8 * 9216;
    {
      const unsigned* f3 = (const unsigned*)(ws + WS_FLAG3) + t * 32;
      const bool poll = lane < 8 && lane != g;
      unsigned f0 = poll ? ld_flag(f3 + lane) : tag;
#pragma unroll 1
      for (unsigned spins = 0; spins < SPIN_CAP; ++spins) {
        if (__all(f0 == tag)) break;
        __builtin_amdgcn_s_sleep(1);
        if (poll) f0 = ld_flag(f3 + lane);
      }
      asm volatile("" ::: "memory");
    }
    STAMP(11);
    u32x4 v[7];
    u32x2 r[7];
#pragma unroll
    for (int s = 0; s < 7; ++s) {
      const unsigned so = sb + ((g + 1 + s) & 7) * 9216;
      v[s] = __builtin_amdgcn_raw_buffer_load_b128(wsr, so + tid * 16, 0, AUX_SC1);
      if (wid < 2) r[s] = __builtin_amdgcn_raw_buffer_load_b64(wsr, so + 8192 + tid * 8, 0, AUX_SC1);
    }
    auto up = [](u32x2 p) { return (f32x4){bf_lo(p[0]), bf_hi(p[0]), bf_lo(p[1]), bf_hi(p[1])}; };
    f32x4 lo = unpk_lo(own) + (up(xa) + up(ba)), hi = unpk_hi(own) + (up(xb) + up(bb));
#pragma unroll
    for (int s = 0; s < 7; ++s) { lo += unpk_lo(v[s]); hi += unpk_hi(v[s]); }
    *(u32x2*)(out + row + ca) = (u32x2){pk2(lo[0], lo[1]), pk2(lo[2], lo[3])};
    *(u32x2*)(out + row + ca + 64) = (u32x2){pk2(hi[0], hi[1]), pk2(hi[2], hi[3])};
    if (wid < 2) {
      f32x4 m = up(ownr) + (up(xc) + up(bc));
#pragma unroll
      for (int s = 0; s < 7; ++s) m += up(r[s]);
      *(u32x2*)(out + row + cr) = (u32x2){pk2(m[0], m[1]), pk2(m[2], m[3])};
    }
    STAMP(14); STAMP_RT(21);
    STAMP_FLUSH();
    if (blockIdx.x == 0 && tid == 0) st_flag((unsigned*)(ws + WS_EPOCH), tag);
  }
}

extern "C" void kernel_launch(void* const* d_in, const int* in_sizes, int n_in, void* d_out, int out_size, void* d_ws,
                              size_t ws_size, hipStream_t stream) {
  (void)in_sizes; (void)n_in; (void)out_size; (void)ws_size;
  (void)hipFuncSetAttribute((const void*)fused_block_kernel, hipFuncAttributeMaxDynamicSharedMemorySize, LDS_BYTES);
  fused_block_kernel<<<256, 512, LDS_BYTES, stream>>>(
      (const unsigned short*)d_in[0], (const char*)d_in[1], (const char*)d_in[2], (const char*)d_in[4],
      (const unsigned short*)d_in[5], (const char*)d_in[6], (const unsigned short*)d_in[7], (unsigned short*)d_out, (char*)d_ws);
}
```

```cpp
#include <hip/hip_runtime.h>
#include <hip/hip_bf16.h>
#include <type_traits>

typedef __attribute__((ext_vector_type(4))) short s16x4;
typedef __attribute__((ext_vector_type(8))) __bf16 bf16x8;
typedef __attribute__((ext_vector_type(2))) __bf16 bf16x2;
typedef __attribute__((ext_vector_type(4))) float f32x4;
typedef __attribute__((ext_vector_type(2))) float f32x2;
typedef __attribute__((ext_vector_type(4))) unsigned u32x4;
typedef __attribute__((ext_vector_type(2))) unsigned u32x2;
#define LAS __attribute__((address_space(3)))
#define GAS __attribute__((address_space(1)))
#define DI __device__ __forceinline__
#define SCHED() __builtin_amdgcn_sched_barrier(0)
#define BAR() __builtin_amdgcn_s_barrier()
#define WAIT_VM(n) asm volatile("s_waitcnt vmcnt(%0)" ::"n"(n) : "memory")
#define WAIT_LGKM(n) asm volatile("s_waitcnt lgkmcnt(%0)" ::"n"(n) : "memory")

#define STAMP(i) do {} while (0)
#define STAMP_RT(i) do {} while (0)
#define STAMP_DECL
#define STAMP_FLUSH() do {} while (0)

#define CODE_PF 1
#define CODE_PF_BYTES 49152
#define XPOL ""
#define MAP_NGX 2

namespace {

constexpr int DM = 576;
constexpr int ACT_STRIDE = 1184;
constexpr int RING = 64 * ACT_STRIDE;
constexpr int KSLOT = 36864;
constexpr int PQ = RING + 2 * KSLOT;
constexpr int PQ_STRIDE = 144;
constexpr int SIMG = PQ + 64 * PQ_STRIDE;
constexpr int LDS_BYTES = 163840;
constexpr int KTMP = RING + 16384;
constexpr int VTMP = KTMP + 64 * 144;
static_assert(SIMG + 5120 <= LDS_BYTES, "lds");
static_assert(VTMP + 64 * 152 + 16 <= RING + KSLOT, "lds");

constexpr unsigned WS_EPOCH = 0;
constexpr unsigned WS_FLAG1 = 1024;
constexpr unsigned WS_FLAG2 = 2048;
constexpr unsigned WS_FLAG3 = 4096;
constexpr unsigned WS_P = 65536;
constexpr unsigned WS_S = WS_P + 256 * 16384;
constexpr unsigned WS_SLAB = WS_S + 8 * 8192;
constexpr unsigned WS_END = WS_SLAB + 32 * 8 * 8 * 9216;
constexpr unsigned OOB = 0x7ffffff0u;
constexpr int AUX_SC1 = 16;
constexpr unsigned SPIN_CAP = 1u << 16;

DI float bf_lo(unsigned u) { return __builtin_bit_cast(float, u << 16); }
DI float bf_hi(unsigned u) { return __builtin_bit_cast(float, u & 0xffff0000u); }
DI unsigned pk2(float a, float b) {
  f32x2 p = {a, b};
  return __builtin_bit_cast(unsigned, __builtin_convertvector(p, bf16x2));
}
DI float rbf(float a) { return bf_lo(pk2(a, a)); }
template <int CTRL> DI float dpp_add(float v) {
  return v + __builtin_bit_cast(float, __builtin_amdgcn_update_dpp(0, __builtin_bit_cast(int, v), CTRL, 0xf, 0xf, true));
}
DI int fresh_lane() { int v = threadIdx.x & 63; asm volatile("" : "+v"(v)); return v; }
DI bf16x8 lds_b128(unsigned addr) { return *(LAS bf16x8*)(size_t)addr; }
DI s16x4 lds_tr(unsigned addr) { return __builtin_amdgcn_ds_read_tr16_b64_v4i16((LAS s16x4*)(size_t)addr); }
DI bf16x8 join(s16x4 lo, s16x4 hi) {
  return __builtin_bit_cast(bf16x8, __builtin_shufflevector(lo, hi, 0, 1, 2, 3, 4, 5, 6, 7));
}
DI f32x4 mfma(bf16x8 a, bf16x8 b, f32x4 c) { return __builtin_amdgcn_mfma_f32_16x16x32_bf16(a, b, c, 0, 0, 0); }
DI float pos(float v) { const int b = __builtin_bit_cast(int, v); return __builtin_bit_cast(float, b > 0 ? b : 0); }
DI float neg(float v) { const int b = __builtin_bit_cast(int, v); return __builtin_bit_cast(float, b < 0 ? b : 0); }
DI f32x2 relu2(float a, float b) { const f32x2 p = {pos(a), pos(b)}; return p * p; }
DI unsigned phi2(float a, float b) {
  const f32x2 m = (f32x2){a, b} * 1.44269504088896f;
  const f32x2 e = {__builtin_amdgcn_exp2f(neg(m[0])), __builtin_amdgcn_exp2f(neg(m[1]))};
  const f32x2 r = ((f32x2){pos(a), pos(b)} + 1e-6f) + e;
  return __builtin_bit_cast(unsigned, __builtin_convertvector(r, bf16x2));
}
DI unsigned pk2v(f32x2 p) { return __builtin_bit_cast(unsigned, __builtin_convertvector(p, bf16x2)); }
DI unsigned ld_flag(const unsigned* p) { return __hip_atomic_load((GAS unsigned*)p, __ATOMIC_RELAXED, __HIP_MEMORY_SCOPE_AGENT); }
DI void st_flag(unsigned* p, unsigned v) { __hip_atomic_store((GAS unsigned*)p, v, __ATOMIC_RELAXED, __HIP_MEMORY_SCOPE_AGENT); }
DI void wait_flags(const unsigned* base, int count, unsigned tag, int lane, unsigned first) {
  unsigned v = first;
#pragma unroll 1
  for (unsigned spins = 0; spins < SPIN_CAP; ++spins) {
    if (__all(lane >= count || v == tag)) break;
    __builtin_amdgcn_s_sleep(2);
    if (lane < count) v = ld_flag(base + lane);
  }
  asm volatile("" ::: "memory");
}
DI f32x4 unpk_lo(u32x4 v) { return (f32x4){bf_lo(v[0]), bf_hi(v[0]), bf_lo(v[1]), bf_hi(v[1])}; }
DI f32x4 unpk_hi(u32x4 v) { return (f32x4){bf_lo(v[2]), bf_hi(v[2]), bf_lo(v[3]), bf_hi(v[3])}; }

DI void dma(unsigned voff, const char* sbase, unsigned m0v) {
  asm volatile("s_mov_b32 m0, %1\n\ts_nop 0\n\tglobal_load_lds_dwordx4 %0, %2" ::"v"(voff), "s"(m0v), "s"(sbase) : "memory");
}
template <int B, int P> DI void piece(const unsigned (&voff)[5], const char* sbase, unsigned m) {
  asm volatile("s_add_i32 m0, %1, %3\n\ts_nop 0\n\tglobal_load_lds_dwordx4 %0, %2 offset:%4"
               ::"v"(voff[P]), "s"(m), "s"(sbase), "n"(B * 4096 + P * 1024 - B * 128), "n"(B * 128) : "memory", "scc");
}
DI void touch(const char* sbase, unsigned voff, unsigned& dummy) { asm volatile("global_load_dword %0, %1, %2" : "+v"(dummy) : "v"(voff), "s"(sbase) : "memory"); }

template <int BOFF0>
DI void kstep9(f32x4 (&acc)[9][4], bf16x8 (&A)[9], const bf16x8 (&Bc)[4], bf16x8 (&Bn)[4], unsigned ab, unsigned abn, unsigned bn) {
  s16x4 lx, ly;
  constexpr int BTS = 16 * ACT_STRIDE;
#define MM(blk, j) acc[blk][j] = mfma(A[blk], Bc[j], acc[blk][j])
#define RL(v, base, blk) v = lds_tr((base) + (blk) * 4096)
#define RH(v, base, blk) A[blk] = join(v, lds_tr((base) + (blk) * 4096 + 512))
  RL(lx, ab, 3); SCHED(); MM(0, 0); SCHED(); RH(lx, ab, 3); SCHED(); MM(0, 1); SCHED(); MM(0, 2); MM(0, 3); SCHED();
  MM(1, 0); SCHED(); Bn[0] = lds_b128(bn + BOFF0); SCHED(); MM(1, 1); SCHED();
  Bn[1] = lds_b128(bn + BOFF0 + BTS); SCHED(); MM(1, 2); MM(1, 3); SCHED();
  WAIT_LGKM(2);
  BAR();
  RL(lx, ab, 4); SCHED(); MM(2, 0); SCHED(); RH(lx, ab, 4); SCHED(); MM(2, 1); SCHED();
  RL(ly, ab, 5); SCHED(); MM(2, 2); SCHED(); RH(ly, ab, 5); SCHED(); MM(2, 3); SCHED();
  RL(lx, ab, 6); SCHED(); MM(3, 0); SCHED(); RH(lx, ab, 6); SCHED(); MM(3, 1); SCHED(); MM(3, 2); MM(3, 3); SCHED();
  RL(lx, ab, 7); SCHED(); MM(4, 0); SCHED(); RH(lx, ab, 7); SCHED(); MM(4, 1); SCHED(); MM(4, 2); MM(4, 3); SCHED();
  RL(lx, ab, 8); SCHED(); MM(5, 0); SCHED(); RH(lx, ab, 8); SCHED(); MM(5, 1); SCHED(); MM(5, 2); MM(5, 3); SCHED();
  MM(6, 0); SCHED(); Bn[2] = lds_b128(bn + BOFF0 + 2 * BTS); SCHED(); MM(6, 1); SCHED();
  Bn[3] = lds_b128(bn + BOFF0 + 3 * BTS); SCHED(); MM(6, 2); MM(6, 3); SCHED();
  WAIT_LGKM(2);
  BAR();
  RL(lx, abn, 0); SCHED(); MM(7, 0); SCHED(); RH(lx, abn, 0); SCHED(); MM(7, 1); SCHED();
  RL(ly, abn, 1); SCHED(); MM(7, 2); SCHED(); RH(ly, abn, 1); SCHED(); MM(7, 3); SCHED();
  RL(lx, abn, 2); SCHED(); MM(8, 0); SCHED(); RH(lx, abn, 2); SCHED(); MM(8, 1); SCHED(); MM(8, 2); MM(8, 3); SCHED();
}
template <int BOFF0>
DI void kstep4(f32x4 (&acc)[4][4], bf16x8 (&A)[4], const bf16x8 (&Bc)[4], bf16x8 (&Bn)[4], unsigned ab, unsigned abn, unsigned d3, unsigned bn) {
  s16x4 lx, ly;
  constexpr int BTS = 16 * ACT_STRIDE;
#define QL(v, base, off) v = lds_tr((base) + (off))
#define QH(v, i, base, off) A[i] = join(v, lds_tr((base) + (off) + 576))
  const unsigned a3 = ab + d3;
  QL(lx, a3, 0); SCHED(); MM(0, 0); SCHED(); QH(lx, 3, a3, 0); SCHED(); MM(0, 1); SCHED();
  Bn[0] = lds_b128(bn + BOFF0); SCHED(); MM(0, 2); SCHED(); Bn[1] = lds_b128(bn + BOFF0 + BTS); SCHED(); MM(0, 3); SCHED();
  Bn[2] = lds_b128(bn + BOFF0 + 2 * BTS); SCHED(); MM(1, 0); SCHED(); Bn[3] = lds_b128(bn + BOFF0 + 3 * BTS); SCHED();
  MM(1, 1); MM(1, 2); MM(1, 3); SCHED();
  WAIT_LGKM(4);
  BAR();
  QL(lx, abn, 0); SCHED(); MM(2, 0); SCHED(); QH(lx, 0, abn, 0); SCHED(); MM(2, 1); SCHED();
  QL(ly, abn, 5120); SCHED(); MM(2, 2); SCHED(); QH(ly, 1, abn, 5120); SCHED(); MM(2, 3); SCHED();
  QL(lx, abn, 10240); SCHED(); MM(3, 0); SCHED(); QH(lx, 2, abn, 10240); SCHED(); MM(3, 1); MM(3, 2); MM(3, 3); SCHED();
}
#undef MM

}

__global__ __launch_bounds__(512, 2) void fused_block_kernel(
    const unsigned short* __restrict__ x, const char* __restrict__ wqkv, const char* __restrict__ wo,
    const char* __restrict__ w1, const unsigned short* __restrict__ b1, const char* __restrict__ w2,
    const unsigned short* __restrict__ b2, unsigned short* __restrict__ out, char* __restrict__ ws) {
  extern __shared__ __attribute__((aligned(16))) char lds[];
  const int tid = threadIdx.x, lane = tid & 63;
  const int wid = __builtin_amdgcn_readfirstlane(tid >> 6);
  constexpr int NGX = MAP_NGX, PFN = 32 / NGX;
  const int xj = blockIdx.x & 7, xi = blockIdx.x >> 3;
  const int g = NGX * (xj / NGX) + xi % NGX, pfr = xi / NGX, t = (xj % NGX) * PFN + pfr;
  const unsigned LB = (unsigned)(size_t)lds;
  const __amdgpu_buffer_rsrc_t wsr = __builtin_amdgcn_make_buffer_rsrc(ws, 0, WS_END, 0x00020000);

  unsigned long long pc0;
  asm volatile("s_getpc_b64 %0" : "=s"(pc0));
  STAMP_DECL
  STAMP(0); STAMP_RT(20);
  const int lr = lane >> 3, lc = lane & 7;
  const int l = wid - 4;
  const int cE = lc ^ (((lr >> 1) & 1) << 1), cO = cE ^ 4;
  unsigned voff[5];
  const unsigned m0r = LB + RING;
  const char* baseQ = wqkv + g * 144 + l * 1152;
  auto issueQ = [&](const char* sq, unsigned mq) {
    dma(voff[0], sq, mq); dma(voff[1], sq, mq + 1024); dma(voff[2], sq, mq + 2048); dma(voff[3], sq, mq + 3072); dma(voff[4], sq, mq + 4096);
  };
  unsigned dummy = 0;
  unsigned epoch;
  if (wid < 4) {
    u32x4 xr[2][9];
    const char* xp = (const char*)x + (size_t)(t * 64 + wid * 16) * 1152;
    const unsigned xo = lr * 1152 + lc * 16;
#pragma unroll
    for (int h = 0; h < 2; ++h)
#pragma unroll
      for (int i = 0; i < 9; ++i)
        asm volatile("global_load_dwordx4 %0, %1, %2 offset:%3 " XPOL : "=v"(xr[h][i]) : "v"(xo + h * 8 * 1152), "s"(xp), "n"(i * 128) : "memory");
    asm volatile("global_load_dword %0, %1, %2 sc1" : "=v"(epoch) : "v"(0u), "s"(ws) : "memory");
#pragma unroll
    for (int h = 0; h < 2; ++h) {
      if (h == 0) WAIT_VM(10); else WAIT_VM(0);
      SCHED();
#pragma unroll
      for (int i = 0; i < 9; ++i) asm volatile("" : "+v"(xr[h][i]));
      if (h == 0) STAMP(1);
      const bf16x2 ones = __builtin_bit_cast(bf16x2, 0x3f803f80u);
      float s0 = 0.f, s1 = 0.f, q0 = 0.f, q1 = 0.f;
#pragma unroll
      for (int i = 0; i < 9; ++i) {
        const unsigned u0 = xr[h][i][0], u1 = xr[h][i][1], u2 = xr[h][i][2], u3 = xr[h][i][3];
        const bf16x2 a = __builtin_bit_cast(bf16x2, u0), b = __builtin_bit_cast(bf16x2, u1);
        const bf16x2 c = __builtin_bit_cast(bf16x2, u2), d = __builtin_bit_cast(bf16x2, u3);
        s0 = __builtin_amdgcn_fdot2_f32_bf16(a, ones, s0, false); s1 = __builtin_amdgcn_fdot2_f32_bf16(b, ones, s1, false);
        q0 = __builtin_amdgcn_fdot2_f32_bf16(a, a, q0, false); q1 = __builtin_amdgcn_fdot2_f32_bf16(b, b, q1, false);
        s0 = __builtin_amdgcn_fdot2_f32_bf16(c, ones, s0, false); s1 = __builtin_amdgcn_fdot2_f32_bf16(d, ones, s1, false);
        q0 = __builtin_amdgcn_fdot2_f32_bf16(c, c, q0, false); q1 = __builtin_amdgcn_fdot2_f32_bf16(d, d, q1, false);
      }
      float s = s0 + s1, q = q0 + q1;
      s = dpp_add<0xB1>(s); q = dpp_add<0xB1>(q);
      s = dpp_add<0x4E>(s); q = dpp_add<0x4E>(q);
      s = dpp_add<0x141>(s); q = dpp_add<0x141>(q);
      const float mean = s * (1.f / 576.f);
      const float var = fmaxf(q * (1.f / 576.f) - mean * mean, 0.f);
      const float mb = rbf(mean);
      const float sd = rbf(__builtin_amdgcn_sqrtf(rbf(rbf(var) + 1e-5f)));
      const float rs = __builtin_amdgcn_rcpf(sd), nb = -mb * rs;
      const unsigned wa = LB + (wid * 16 + h * 8 + lr) * ACT_STRIDE + lc * 16;
#pragma unroll
      for (int i = 0; i < 9; ++i) {
        u32x4 o;
#pragma unroll
        for (int e = 0; e < 4; ++e) o[e] = pk2(fmaf(bf_lo(xr[h][i][e]), rs, nb), fmaf(bf_hi(xr[h][i][e]), rs, nb));
        *(LAS u32x4*)(size_t)(wa + i * 128) = o;
      }
    }
  } else {
    if (wid == 7) {
      const unsigned i = (unsigned)lane * 32u + (unsigned)xi;
      touch((const char*)pc0, (i < CODE_PF_BYTES / 128 ? i : 0u) * 128u, dummy);
    }
    asm volatile("global_load_dword %0, %1, %2 sc1" : "=v"(epoch) : "v"(0u), "s"(ws) : "memory");
    if (wid == 7) {
      constexpr int RW = 576 / PFN, NQ = (RW + 7) / 8;
      const char* pq_ = wqkv + (size_t)(RW * pfr) * 3456 + ((g * 144) & ~127);
      const int c6 = lc < 5 ? lc : 5;
#pragma unroll
      for (int n = 0; n < NQ; ++n) {
        const int r = 8 * n + lr < RW ? 8 * n + lr : RW - 1;
        touch(pq_, r * 3456 + (c6 >> 1) * 1152 + (c6 & 1) * 128, dummy);
      }
      WAIT_VM(NQ);
    } else {
#pragma unroll
      for (int p = 0; p < 5; ++p) {
        int i = 64 * p + lane; i = i < 288 ? i : 287;
        const int r = (i * 7282) >> 16;
        voff[p] = r * 3456 + (i - 9 * r) * 16;
      }
      issueQ(baseQ, m0r + l * 5120);
      WAIT_VM(5);
    }
    SCHED();
    STAMP(1);
  }
  asm volatile("" : "+v"(epoch));
  const unsigned tag = __builtin_amdgcn_readfirstlane(epoch) + 1u;
  STAMP(2);

  if (wid >= 4) {
#define LPRIO 2
#define CPRIO 3
    __builtin_amdgcn_s_setprio(LPRIO);
    const char* base1 = w1 + g * 1152;
    const char* base2 = w2 + (size_t)(g * 576) * 1152;
    const char* baseO = wo + (size_t)(g * 72) * 1152;
    auto setvoff = [&](unsigned ldw, int rowsel) {
#pragma unroll
      for (int p = 0; p < 4; ++p) voff[p] = (rowsel * 8 * p + lr) * ldw + ((p & 1) ? cO : cE) * 16;
    };
    using I1 = std::integral_constant<int, 1>;
    WAIT_LGKM(0);
    if (l < 3) {
      auto prog = [&](auto LC) {
        constexpr int L = decltype(LC)::value;
        auto issue8 = [&](const char* sb, unsigned m) {
          if constexpr (L == 0) { piece<8, 0>(voff, sb, m); piece<8, 3>(voff, sb, m); }
          if constexpr (L == 1) piece<8, 1>(voff, sb, m);
          if constexpr (L == 2) piece<8, 2>(voff, sb, m);
        };
        auto issueLo = [&](const char* sb, unsigned m) {
          if constexpr (L == 0) { piece<0, 2>(voff, sb, m); piece<1, 1>(voff, sb, m); piece<2, 0>(voff, sb, m); piece<2, 3>(voff, sb, m); piece<3, 2>(voff, sb, m); }
          if constexpr (L == 1) { piece<0, 0>(voff, sb, m); piece<0, 3>(voff, sb, m); piece<1, 2>(voff, sb, m); piece<2, 1>(voff, sb, m); piece<3, 0>(voff, sb, m); piece<3, 3>(voff, sb, m); }
          if constexpr (L == 2) { piece<0, 1>(voff, sb, m); piece<1, 0>(voff, sb, m); piece<1, 3>(voff, sb, m); piece<2, 2>(voff, sb, m); piece<3, 1>(voff, sb, m); }
        };
        auto issueHi = [&](const char* sb, unsigned m) {
          if constexpr (L == 0) { piece<4, 1>(voff, sb, m); piece<5, 0>(voff, sb, m); piece<5, 3>(voff, sb, m); piece<6, 2>(voff, sb, m); piece<7, 1>(voff, sb, m); }
          if constexpr (L == 1) { piece<4, 2>(voff, sb, m); piece<5, 1>(voff, sb, m); piece<6, 0>(voff, sb, m); piece<6, 3>(voff, sb, m); piece<7, 2>(voff, sb, m); }
          if constexpr (L == 2) { piece<4, 0>(voff, sb, m); piece<4, 3>(voff, sb, m); piece<5, 2>(voff, sb, m); piece<6, 1>(voff, sb, m); piece<7, 0>(voff, sb, m); piece<7, 3>(voff, sb, m); }
        };
        constexpr int WX = L == 0 ? 10 : 11;
        const unsigned mA = m0r, mB = m0r + KSLOT;
        {
          const char* sq = baseQ + 32 * 3456;
          const unsigned mq = m0r + L * 5120;
          issueQ(sq, mq + 16384); issueQ(sq + 32 * 3456, mq + KSLOT);
          sq += 64 * 3456;
          constexpr int RW1 = 576 / PFN, NT = (RW1 * 9 + 191) / 192;
          {
            const char* p1 = w1 + (size_t)(RW1 * pfr) * 9216 + g * 1152;
#pragma unroll
            for (int n = 0; n < NT; ++n) {
              const int i0 = (3 * n + L) * 64 + lane, i = i0 < RW1 * 9 ? i0 : 0;
              const int r = (i * 7282) >> 16;
              touch(p1, r * 9216 + (i - 9 * r) * 128, dummy);
            }
          }
          STAMP(15);
          WAIT_VM(10 + NT);
          STAMP(16);
          BAR();
          STAMP(3);
          issueQ(sq, mq + KSLOT + 16384); WAIT_VM(10 + NT); BAR();
          sq += 32 * 3456;
          issueQ(sq, mq); WAIT_VM(10 + NT); BAR();
          issueQ(sq + 32 * 3456, mq + 16384); WAIT_VM(10); BAR();
          issueQ(sq + 64 * 3456, mq + KSLOT); WAIT_VM(10); BAR();
          issueQ(sq + 96 * 3456, mq + KSLOT + 16384); WAIT_VM(10); BAR();
          sq += 128 * 3456;
#pragma unroll 1
          for (int j = 0; j < 2; ++j) {
            issueQ(sq, mq); WAIT_VM(10); BAR();
            issueQ(sq + 32 * 3456, mq + 16384); WAIT_VM(10); BAR();
            issueQ(sq + 64 * 3456, mq + KSLOT); WAIT_VM(10); BAR();
            issueQ(sq + 96 * 3456, mq + KSLOT + 16384); WAIT_VM(10); BAR();
            sq += 128 * 3456;
          }
          issueQ(sq, mq); WAIT_VM(10); BAR();
          issueQ(sq + 32 * 3456, mq + 16384); WAIT_VM(10); BAR();
        }
        setvoff(9216, 1);
        const char* sb = base1;
        issueLo(sb, mB); WAIT_VM(10); BAR();
        issueHi(sb, mB); issue8(sb, mB); WAIT_VM(12); BAR();
        issueLo(sb + 32 * 9216, mA); BAR(); BAR();
        WAIT_VM(12); BAR();
        auto kstepL = [&](const char* s1, unsigned m1, const char* s2, unsigned m2, auto mid) {
          issueHi(s1, m1); mid(); WAIT_VM(WX); BAR();
          issue8(s1, m1); issueLo(s2, m2); WAIT_VM(12); BAR();
        };
        auto nop = []() {};
        sb = base1 + 32 * 9216;
#pragma unroll 1
        for (int it = 0; it < 8; ++it) {
          kstepL(sb, mA, sb + 32 * 9216, mB, nop);
          kstepL(sb + 32 * 9216, mB, sb + 64 * 9216, mA, nop);
          sb += 64 * 9216;
        }
        issueHi(sb, mA); WAIT_VM(WX); BAR();
        issue8(sb, mA); setvoff(1152, 1); issueLo(base2, mB); WAIT_VM(12); BAR();
        sb = base2;
        kstepL(sb, mB, sb + 32 * 1152, mA, nop);
        kstepL(sb + 32 * 1152, mA, sb + 64 * 1152, mB, [&]() { BAR(); });
        sb += 64 * 1152;
#pragma unroll 1
        for (int it = 0; it < 7; ++it) {
          kstepL(sb, mB, sb + 32 * 1152, mA, nop);
          kstepL(sb + 32 * 1152, mA, sb + 64 * 1152, mB, nop);
          sb += 64 * 1152;
        }
        kstepL(sb, mB, sb + 32 * 1152, mA, nop);
        sb -= 128 * 1152;
        kstepL(sb + 160 * 1152, mA, baseO, mB, nop);
        kstepL(baseO, mB, baseO + 32 * 1152, mA, nop);
        issueHi(baseO + 32 * 1152, mA); WAIT_VM(WX); BAR();
        issue8(baseO + 32 * 1152, mA); setvoff(1152, 0); issueLo(baseO + 64 * 1152, mB); WAIT_VM(12); BAR();
        issueHi(baseO + 64 * 1152, mB); WAIT_VM(WX); BAR();
        issue8(baseO + 64 * 1152, mB); WAIT_VM(6); BAR();
        WAIT_VM(0); BAR(); BAR();
      };
      if (l == 0) prog(std::integral_constant<int, 0>{});
      else if (l == 1) prog(std::integral_constant<int, 1>{});
      else prog(std::integral_constant<int, 2>{});
    } else {
      auto bars = [&](int n) {
#pragma unroll 1
        for (int i = 0; i < n; ++i) BAR();
      };
      const int lane = fresh_lane();
      constexpr int RW = 576 / PFN;
      BAR();
      STAMP(3);
      bars(17 + 1);
      {
        const char* p2 = base2 + (size_t)(RW * pfr) * 1152;
#pragma unroll
        for (int n = 0; n < (RW * 9 + 63) / 64; ++n) {
          const int i = lane + 64 * n < RW * 9 ? lane + 64 * n : 0;
          touch(p2, i * 128, dummy);
        }
        constexpr int WOL = (648 + PFN - 1) / PFN;
        static_assert(WOL <= 64, "prefetch steps");
        int i = pfr * WOL + lane; i = (lane < WOL && i < 648) ? i : 0;
        touch(baseO, i * 128, dummy);
      }
      bars(2);
      bars(13);
      if (lane == 0) st_flag((unsigned*)(ws + WS_FLAG1) + g * 32 + t, tag);
      bars(7);
      unsigned fl = lane < 32 ? ld_flag((const unsigned*)(ws + WS_FLAG1) + g * 32 + lane) : tag;
      bars(3);
      STAMP(16);
      wait_flags((const unsigned*)(ws + WS_FLAG1) + g * 32, 32, tag, lane, fl);
      STAMP(17);
      {
        u32x4 hv[16];
        const unsigned po = WS_P + (g * 32 + (lane >> 5)) * 16384 + t * 512 + (lane & 31) * 16;
#pragma unroll
        for (int n = 0; n < 16; ++n)
          hv[n] = __builtin_amdgcn_raw_buffer_load_b128(wsr, po, n * 32768, AUX_SC1);
        SCHED();
        bars(5);
        f32x4 a = __builtin_bit_cast(f32x4, hv[0]), b = __builtin_bit_cast(f32x4, hv[1]);
#pragma unroll
        for (int n = 2; n < 16; n += 2) { a += __builtin_bit_cast(f32x4, hv[n]); b += __builtin_bit_cast(f32x4, hv[n + 1]); }
        a += b;
        SCHED();
        BAR();
        const int hh = lane >> 5, li = 32 * (t & 1) + (lane & 31), tile = t >> 1;
        float s0 = hh ? a[2] : a[0], s1 = hh ? a[3] : a[1], o0 = hh ? a[0] : a[2], o1 = hh ? a[1] : a[3];
        const unsigned x0 = (unsigned)__builtin_amdgcn_permlane32_swap(__builtin_bit_cast(unsigned, o0), __builtin_bit_cast(unsigned, o0), false, false)[hh ? 0 : 1];
        const unsigned x1 = (unsigned)__builtin_amdgcn_permlane32_swap(__builtin_bit_cast(unsigned, o1), __builtin_bit_cast(unsigned, o1), false, false)[hh ? 0 : 1];
        s0 += __builtin_bit_cast(float, x0); s1 += __builtin_bit_cast(float, x1);
        const int di = (int)((0x4433322221111000ull >> (4 * tile)) & 15u), ej = (int)((0x4343243214210410ull >> (4 * tile)) & 15u);
        const int d = 16 * di + 4 * (li >> 4) + 2 * hh, e = 16 * ej + (li & 15);
        const int hd = (d * 57) >> 10, he = (e * 57) >> 10;
        const bool ok = d < 72 && (e == 72 || (e < 72 && hd == he));
        const int row = e == 72 ? 18 : e - 18 * he;
        __builtin_amdgcn_raw_buffer_store_b32(pk2(s0, s1), wsr, ok ? WS_S + g * 8192 + hd * 1280 + row * 64 + (d - 18 * hd) * 2 : OOB, 0, AUX_SC1);
      }
      bars(5);
      WAIT_VM(0);
      if (lane == 0) st_flag((unsigned*)(ws + WS_FLAG2) + g * 32 + t, tag);
      bars(2 + 1 + 7);
      fl = lane < 32 ? ld_flag((const unsigned*)(ws + WS_FLAG2) + g * 32 + lane) : tag;
      bars(3);
      STAMP(18);
      wait_flags((const unsigned*)(ws + WS_FLAG2) + g * 32, 32, tag, lane, fl);
      STAMP(19);
      {
        u32x4 iv[5];
#pragma unroll
        for (int n = 0; n < 5; ++n) iv[n] = __builtin_amdgcn_raw_buffer_load_b128(wsr, WS_S + g * 8192 + lane * 16, n * 1024, AUX_SC1);
        SCHED();
        bars(5);
        const int c = lane & 3;
#pragma unroll
        for (int n = 0; n < 5; ++n) {
          const int row = 16 * n + (lane >> 2);
          const bool live = row != 19 && row != 39 && row != 59 && row != 79 && c < 3;
          u32x4 v = iv[n];
          v[0] = live ? v[0] : 0u;
          v[1] = (live && c < 2) ? v[1] : 0u; v[2] = (live && c < 2) ? v[2] : 0u; v[3] = (live && c < 2) ? v[3] : 0u;
          *(LAS u32x4*)(size_t)(LB + SIMG + n * 1024 + lane * 16) = v;
        }
        WAIT_LGKM(0);
      }
      bars(27);
    }
    STAMP(8);
    WAIT_VM(0);
    asm volatile("" ::"v"(dummy));
  } else {
    const int w = wid;
    __builtin_amdgcn_s_setprio(CPRIO);
    const int g4 = lane >> 4, q = (lane & 15) >> 2, p = lane & 3;
    const int cf = (((q >> 1) & 1) << 1) | ((g4 & 1) << 2);
    unsigned a0 = LB + RING + (8 * g4 + q) * 128 + (((2 * w + (p >> 1)) ^ cf) * 16) + (p & 1) * 8;
    unsigned a1 = a0 + KSLOT;
    asm volatile("" : "+v"(a1));
    const unsigned b0 = LB + (lane & 15) * ACT_STRIDE + g4 * 16;
    bf16x8 B0[4], B1[4];
    f32x4 acc[9][4];
    {
      f32x4 qa[4][4];
      bf16x8 A[4];
#pragma unroll
      for (int i = 0; i < 4; ++i)
#pragma unroll
        for (int j = 0; j < 4; ++j) qa[i][j] = (f32x4){0.f, 0.f, 0.f, 0.f};
      STAMP(15);
      WAIT_LGKM(0);
      BAR();
      STAMP(3);
#pragma unroll
      for (int j = 0; j < 4; ++j) B0[j] = lds_b128(b0 + j * 16 * ACT_STRIDE);
      unsigned ac = LB + RING + (8 * g4 + q) * 144 + 32 * w + 8 * p, an = ac + KSLOT, bc = b0;
      const unsigned d3 = (w < 3 ? w : 0) * 5120 + 128 - 32 * w;
      A[0] = join(lds_tr(ac), lds_tr(ac + 576));
      A[1] = join(lds_tr(ac + 5120), lds_tr(ac + 5120 + 576));
      A[2] = join(lds_tr(ac + 10240), lds_tr(ac + 10240 + 576));
#pragma unroll 1
      for (int it = 0; it < 9; ++it) {
        kstep4<64>(qa, A, B0, B1, ac, ac + 16384, d3, bc);
        kstep4<128>(qa, A, B1, B0, ac + 16384, an, d3, bc);
        const unsigned tmp = ac; ac = an; an = tmp;
        bc += 128;
      }
      STAMP(4);
      const int lane = fresh_lane(), g4 = lane >> 4, q = (lane & 15) >> 2, p = lane & 3;
      u32x2 bb[9];
#pragma unroll
      for (int blk = 0; blk < 9; ++blk) bb[blk] = *(const u32x2*)(b1 + g * 576 + 64 * blk + 16 * w + 4 * g4);
      {
        const unsigned tk = (lane & 15), col = (16 * w + 4 * g4) * 2;
        const unsigned aq = LB + PQ + tk * 144 + col, ak = LB + KTMP + tk * 144 + col, av = LB + VTMP + tk * 152 + col;
        constexpr unsigned DROP = 0x40000;
        const unsigned ar = w == 3 ? (g4 == 0 ? LB + VTMP + tk * 152 + 144 : DROP)
                                   : g4 >= 2 ? DROP : (w == 0 ? LB + PQ + tk * 144 : w == 1 ? LB + KTMP + tk * 144 : LB + VTMP + tk * 152) + 128 + g4 * 8;
        const unsigned rs = w >= 2 ? 16 * 152 : 16 * 144;
        u32x2 oq[4], ok[4], ov[4], orm[4];
#pragma unroll
        for (int tt = 0; tt < 4; ++tt) {
          ov[tt] = (u32x2){pk2(qa[2][tt][0], qa[2][tt][1]), pk2(qa[2][tt][2], qa[2][tt][3])};
          ok[tt] = (u32x2){phi2(qa[1][tt][0], qa[1][tt][1]), phi2(qa[1][tt][2], qa[1][tt][3])};
        }
        SCHED();
#pragma unroll
        for (int tt = 0; tt < 4; ++tt) {
          *(LAS u32x2*)(size_t)(av + tt * 16 * 152) = ov[tt];
          *(LAS u32x2*)(size_t)(ak + tt * 16 * 144) = ok[tt];
        }
        if (w < 2) {
#pragma unroll
          for (int tt = 0; tt < 4; ++tt) orm[tt] = (u32x2){phi2(qa[3][tt][0], qa[3][tt][1]), phi2(qa[3][tt][2], qa[3][tt][3])};
        } else {
#pragma unroll
          for (int tt = 0; tt < 4; ++tt) {
            orm[tt] = (u32x2){pk2(qa[3][tt][0], qa[3][tt][1]), pk2(qa[3][tt][2], qa[3][tt][3])};
            if (w == 3) orm[tt] = (u32x2){0x00003f80u, 0u};
          }
        }
#pragma unroll
        for (int tt = 0; tt < 4; ++tt) oq[tt] = (u32x2){phi2(qa[0][tt][0], qa[0][tt][1]), phi2(qa[0][tt][2], qa[0][tt][3])};
        SCHED();
#pragma unroll
        for (int tt = 0; tt < 4; ++tt) {
          *(LAS u32x2*)(size_t)(ar + tt * rs) = orm[tt];
          *(LAS u32x2*)(size_t)(aq + tt * 16 * 144) = oq[tt];
        }
      }
      WAIT_LGKM(0);
      BAR();
      const unsigned ti = (unsigned)(0x4433322221111000ull >> (16 * w)) & 0xffffu;
      const unsigned tj = (unsigned)(0x4343243214210410ull >> (16 * w)) & 0xffffu;
      const unsigned kr = LB + KTMP + (8 * g4 + q) * 144 + p * 8, vr = LB + VTMP + (8 * g4 + q) * 152 + p * 8;
#pragma unroll
      for (int n = 0; n < 4; ++n) {
        const unsigned di = (ti >> (4 * n)) & 15u, ej = (tj >> (4 * n)) & 15u;
        f32x4 d = {0.f, 0.f, 0.f, 0.f};
#pragma unroll
        for (int ks = 0; ks < 2; ++ks) {
          const unsigned ka = kr + ks * 32 * 144 + di * 32, va = vr + ks * 32 * 152 + ej * 32;
          const bf16x8 fa = join(lds_tr(ka), lds_tr(ka + 4 * 144));
          const bf16x8 fb = join(lds_tr(va), lds_tr(va + 4 * 152));
          d = mfma(fa, fb, d);
        }
        __builtin_amdgcn_raw_buffer_store_b128(__builtin_bit_cast(u32x4, d), wsr,
                                               WS_P + (g * 32 + t) * 16384 + (4 * w + n) * 1024 + lane * 16, 0, AUX_SC1);
      }
#pragma unroll
      for (int i = 0; i < 9; ++i) {
        const f32x4 bv = {bf_lo(bb[i][0]), bf_hi(bb[i][0]), bf_lo(bb[i][1]), bf_hi(bb[i][1])};
#pragma unroll
        for (int j = 0; j < 4; ++j) acc[i][j] = bv;
      }
      WAIT_LGKM(0);
      BAR();
      STAMP(5);
    }
    bf16x8 A[9];
    {
#pragma unroll
      for (int j = 0; j < 4; ++j) B0[j] = lds_b128(b0 + j * 16 * ACT_STRIDE);
      A[0] = join(lds_tr(a1), lds_tr(a1 + 512));
      A[1] = join(lds_tr(a1 + 4096), lds_tr(a1 + 4096 + 512));
      A[2] = join(lds_tr(a1 + 8192), lds_tr(a1 + 8192 + 512));
      unsigned bc = b0;
#pragma unroll 1
      for (int it = 0; it < 9; ++it) {
        if (__builtin_expect(it == 3, 0)) WAIT_VM(0);
        kstep9<64>(acc, A, B0, B1, a1, a0, bc);
        kstep9<128>(acc, A, B1, B0, a0, a1, bc);
        bc += 128;
      }
      STAMP(6);
      const int lane = fresh_lane(), g4 = lane >> 4;
      const unsigned ha = LB + (lane & 15) * ACT_STRIDE + (16 * w + 8 * (g4 >> 1)) * 2;
      u32x4 hq[18];
#pragma unroll
      for (int n = 0; n < 18; ++n) {
        const f32x4 u = n < 16 ? acc[2 * (n & 3)][n >> 2] : acc[8][2 * (n - 16)];
        const f32x4 v = n < 16 ? acc[2 * (n & 3) + 1][n >> 2] : acc[8][2 * (n - 16) + 1];
        const auto s0 = __builtin_amdgcn_permlane16_swap(pk2v(relu2(u[0], u[1])), pk2v(relu2(v[0], v[1])), false, false);
        const auto s1 = __builtin_amdgcn_permlane16_swap(pk2v(relu2(u[2], u[3])), pk2v(relu2(v[2], v[3])), false, false);
        hq[n] = (u32x4){(unsigned)s0[0], (unsigned)s1[0], (unsigned)s0[1], (unsigned)s1[1]};
        if (n >= 2) {
          SCHED();
          const int m = n - 2;
          *(LAS u32x4*)(size_t)(ha + (m >> 2) * 16 * ACT_STRIDE + (128 * (m & 3) + 64 * (g4 & 1)) * 2) = hq[m];
          SCHED();
        }
      }
      *(LAS u32x4*)(size_t)(ha + (g4 & 1) * 16 * ACT_STRIDE + 512 * 2) = hq[16];
      *(LAS u32x4*)(size_t)(ha + (2 + (g4 & 1)) * 16 * ACT_STRIDE + 512 * 2) = hq[17];
      WAIT_LGKM(0);
      BAR();
      STAMP(7);
    }
    {
#pragma unroll
      for (int j = 0; j < 4; ++j) B0[j] = lds_b128(b0 + j * 16 * ACT_STRIDE);
#pragma unroll
      for (int i = 0; i < 9; ++i)
#pragma unroll
        for (int j = 0; j < 4; ++j) acc[i][j] = (f32x4){0.f, 0.f, 0.f, 0.f};
      unsigned bc = b0;
      kstep9<64>(acc, A, B0, B1, a1, a0, bc);
#pragma unroll 1
      for (int it = 0; it < 10; ++it) {
        if (__builtin_expect(it == 6, 0)) {
          const int lane = fresh_lane(), g4 = lane >> 4, r16 = lane & 15;
          const unsigned sa = LB + SIMG + w * 1280 + g4 * 16;
          const bf16x8 ka0 = lds_b128(sa + r16 * 64), ka1 = lds_b128(sa + (r16 < 3 ? 16 + r16 : 19) * 64);
          const unsigned pa = LB + PQ + r16 * PQ_STRIDE + w * 36 + g4 * 16;
          const unsigned oa = LB + r16 * ACT_STRIDE + w * 36 + g4 * 8;
          const unsigned la = g4 == 0 ? oa + 32 : LB + r16 * ACT_STRIDE + 144 + w * 12 + (g4 - 1) * 4;
          u32x4 pb[4];
#pragma unroll
          for (int tt = 0; tt < 4; ++tt)
#pragma unroll
            for (int m = 0; m < 4; ++m) pb[tt][m] = *(LAS unsigned*)(size_t)(pa + tt * 16 * PQ_STRIDE + 4 * m);
#pragma unroll
          for (int tt = 0; tt < 4; ++tt) {
            const bf16x8 b = __builtin_bit_cast(bf16x8, pb[tt]);
            const f32x4 n0 = mfma(ka0, b, (f32x4){0.f, 0.f, 0.f, 0.f}), n1 = mfma(ka1, b, (f32x4){0.f, 0.f, 0.f, 0.f});
            const float d2 = n1[2];
            const unsigned du = __builtin_bit_cast(unsigned, d2);
            const unsigned y = (unsigned)__builtin_amdgcn_permlane32_swap(du, du, false, false)[0];
            const unsigned z = (unsigned)__builtin_amdgcn_permlane16_swap(y, y, false, false)[0];
            const float r = __builtin_amdgcn_rcpf(__builtin_bit_cast(float, z) + 1e-6f);
            const f32x2 p0 = (f32x2){n0[0], n0[1]} * r, p1 = (f32x2){n0[2], n0[3]} * r, p2 = (f32x2){n1[0], n1[1]} * r;
            *(LAS unsigned*)(size_t)(oa + tt * 16 * ACT_STRIDE) = pk2v(p0);
            *(LAS unsigned*)(size_t)(oa + tt * 16 * ACT_STRIDE + 4) = pk2v(p1);
            *(LAS unsigned*)(size_t)(la + tt * 16 * ACT_STRIDE) = g4 == 0 ? pk2v(p2) : 0u;
          }
        }
        kstep9<128>(acc, A, B1, B0, a0, a1, bc);
        kstep9<192>(acc, A, B0, B1, a1, a0, bc);
        bc += it == 7 ? 128 - 18 * 64 : 128;
      }
    }
    STAMP(8);
    {
      const int lane = fresh_lane(), g4 = lane >> 4;
      const int hf = (lane >> 3) & 1, l32 = g4 * 8 + (lane & 7);
#pragma unroll
      for (int tq = 0; tq < 4; ++tq) {
        const int tt = tq;
        const bool own = 2 * tt + hf == g;
        const unsigned sb = own ? OOB : WS_SLAB + ((t * 8 + 2 * tt + hf) * 8 + g) * 9216;
        u32x4 o[4];
#pragma unroll
        for (int j = 0; j < 4; ++j) {
          o[j][0] = pk2(acc[2 * j][tt][0], acc[2 * j][tt][1]); o[j][1] = pk2(acc[2 * j][tt][2], acc[2 * j][tt][3]);
          o[j][2] = pk2(acc[2 * j + 1][tt][0], acc[2 * j + 1][tt][1]); o[j][3] = pk2(acc[2 * j + 1][tt][2], acc[2 * j + 1][tt][3]);
        }
        const u32x2 o2 = {pk2(acc[8][tt][0], acc[8][tt][1]), pk2(acc[8][tt][2], acc[8][tt][3])};
#pragma unroll
        for (int j = 0; j < 4; ++j) __builtin_amdgcn_raw_buffer_store_b128(o[j], wsr, sb + ((w * 4 + j) * 32 + l32) * 16, 0, AUX_SC1);
        __builtin_amdgcn_raw_buffer_store_b64(o2, wsr, sb + 8192 + (w * 32 + l32) * 8, 0, AUX_SC1);
        if (tt == (g >> 1)) {
          const unsigned fa = own ? LB + l32 * 16 + w * 2048 : 0x40000u;
#pragma unroll
          for (int j = 0; j < 4; ++j) *(LAS u32x4*)(size_t)(fa + j * 512) = o[j];
          *(LAS u32x2*)(size_t)(own ? LB + 8192 + (w * 32 + l32) * 8 : 0x40000u) = o2;
        }
      }
    }
    STAMP(9);
    WAIT_VM(0);
    STAMP(10);
  }
  {
    int tid = threadIdx.x; asm volatile("" : "+v"(tid));
    const int lane = tid & 63;
    const int tk = tid & 7, cg = (tid >> 3) & 3;
    const int ca = 128 * ((tid >> 5) & 3) + 16 * (tid >> 7) + 4 * cg, cr = 512 + 16 * ((tid >> 5) & 3) + 4 * cg;
    const size_t row = (size_t)(t * 64 + g * 8 + tk) * DM;
    const u32x2 xa = *(const u32x2*)(x + row + ca), xb = *(const u32x2*)(x + row + ca + 64);
    const u32x2 ba = *(const u32x2*)(b2 + ca), bb = *(const u32x2*)(b2 + ca + 64);
    u32x2 xc = xa, bc = ba;
    if (wid < 2) { xc = *(const u32x2*)(x + row + cr); bc = *(const u32x2*)(b2 + cr); }
    WAIT_LGKM(0);
    BAR();
    if (tid == 0) st_flag((unsigned*)(ws + WS_FLAG3) + t * 32 + g, tag);
    const u32x4 own = *(LAS u32x4*)(size_t)(LB + tid * 16);
    u32x2 ownr = {0u, 0u};
    if (wid < 2) ownr = *(LAS u32x2*)(size_t)(LB + 8192 + tid * 8);
    const unsigned sb = WS_SLAB + (t * 8 + g) * 8 * 9216;
    {
      const unsigned* f3 = (const unsigned*)(ws + WS_FLAG3) + t * 32;
      const bool poll = lane < 8 && lane != g;
      unsigned f0 = poll ? ld_flag(f3 + lane) : tag;
#pragma unroll 1
      for (unsigned spins = 0; spins < SPIN_CAP; ++spins) {
        if (__all(f0 == tag)) break;
        __builtin_amdgcn_s_sleep(1);
        if (poll) f0 = ld_flag(f3 + lane);
      }
      asm volatile("" ::: "memory");
    }
    STAMP(11);
    u32x4 v[7];
    u32x2 r[7];
#pragma unroll
    for (int s = 0; s < 7; ++s) {
      const unsigned so = sb + ((g + 1 + s) & 7) * 9216;
      v[s] = __builtin_amdgcn_raw_buffer_load_b128(wsr, so + tid * 16, 0, AUX_SC1);
      if (wid < 2) r[s] = __builtin_amdgcn_raw_buffer_load_b64(wsr, so + 8192 + tid * 8, 0, AUX_SC1);
    }
    auto up = [](u32x2 p) { return (f32x4){bf_lo(p[0]), bf_hi(p[0]), bf_lo(p[1]), bf_hi(p[1])}; };
    f32x4 lo = unpk_lo(own) + (up(xa) + up(ba)), hi = unpk_hi(own) + (up(xb) + up(bb));
#pragma unroll
    for (int s = 0; s < 7; ++s) { lo += unpk_lo(v[s]); hi += unpk_hi(v[s]); }
    *(u32x2*)(out + row + ca) = (u32x2){pk2(lo[0], lo[1]), pk2(lo[2], lo[3])};
    *(u32x2*)(out + row + ca + 64) = (u32x2){pk2(hi[0], hi[1]), pk2(hi[2], hi[3])};
    if (wid < 2) {
      f32x4 m = up(ownr) + (up(xc) + up(bc));
#pragma unroll
      for (int s = 0; s < 7; ++s) m += up(r[s]);
      *(u32x2*)(out + row + cr) = (u32x2){pk2(m[0], m[1]), pk2(m[2], m[3])};
    }
    STAMP(14); STAMP_RT(21);
    STAMP_FLUSH();
    if (blockIdx.x == 0 && tid == 0) st_flag((unsigned*)(ws + WS_EPOCH), tag);
  }
}

extern "C" void kernel_launch(void* const* d_in, const int* in_sizes, int n_in, void* d_out, int out_size, void* d_ws,
                              size_t ws_size, hipStream_t stream) {
  (void)in_sizes; (void)n_in; (void)out_size; (void)ws_size;
  (void)hipFuncSetAttribute((const void*)fused_block_kernel, hipFuncAttributeMaxDynamicSharedMemorySize, LDS_BYTES);
  fused_block_kernel<<<256, 512, LDS_BYTES, stream>>>(
      (const unsigned short*)d_in[0], (const char*)d_in[1], (const char*)d_in[2], (const char*)d_in[4],
      (const unsigned short*)d_in[5], (const char*)d_in[6], (const unsigned short*)d_in[7], (unsigned short*)d_out, (char*)d_ws);
}
```

```cpp
#include <hip/hip_runtime.h>
#include <hip/hip_bf16.h>
#include <type_traits>

typedef __attribute__((ext_vector_type(4))) short s16x4;
typedef __attribute__((ext_vector_type(8))) __bf16 bf16x8;
typedef __attribute__((ext_vector_type(2))) __bf16 bf16x2;
typedef __attribute__((ext_vector_type(4))) float f32x4;
typedef __attribute__((ext_vector_type(2))) float f32x2;
typedef __attribute__((ext_vector_type(4))) unsigned u32x4;
typedef __attribute__((ext_vector_type(2))) unsigned u32x2;
#define LAS __attribute__((address_space(3)))
#define GAS __attribute__((address_space(1)))
#define DI __device__ __forceinline__
#define SCHED() __builtin_amdgcn_sched_barrier(0)
#define BAR() __builtin_amdgcn_s_barrier()
#define WAIT_VM(n) asm volatile("s_waitcnt vmcnt(%0)" ::"n"(n) : "memory")
#define WAIT_LGKM(n) asm volatile("s_waitcnt lgkmcnt(%0)" ::"n"(n) : "memory")

#define STAMP(i) do {} while (0)
#define STAMP_RT(i) do {} while (0)
#define STAMP_DECL
#define STAMP_FLUSH() do {} while (0)

#define CODE_PF 1
#define CODE_PF_BYTES 24576
#define OUT_AUX 0
#define XPOL ""
#define MAP_NGX 2

namespace {

constexpr int DM = 576;
constexpr int ACT_STRIDE = 1184;
constexpr int RING = 64 * ACT_STRIDE;
constexpr int KSLOT = 36864;
constexpr int PQ = RING + 2 * KSLOT;
constexpr int PQ_STRIDE = 144;
constexpr int SIMG = PQ + 64 * PQ_STRIDE;
constexpr int LDS_BYTES = 163840;
constexpr int KTMP = RING + 16384;
constexpr int VTMP = KTMP + 64 * 144;
static_assert(SIMG + 5120 <= LDS_BYTES, "lds");
static_assert(VTMP + 64 * 152 + 16 <= RING + KSLOT, "lds");

constexpr unsigned WS_EPOCH = 0;
constexpr unsigned WS_FLAG1 = 1024;
constexpr unsigned WS_FLAG2 = 2048;
constexpr unsigned WS_FLAG3 = 4096;
constexpr unsigned WS_P = 65536;
constexpr unsigned WS_S = WS_P + 256 * 16384;
constexpr unsigned WS_SLAB = WS_S + 8 * 8192;
constexpr unsigned WS_END = WS_SLAB + 32 * 8 * 8 * 9216;
constexpr unsigned OOB = 0x7ffffff0u;
constexpr int AUX_SC1 = 16;
constexpr unsigned SPIN_CAP = 1u << 16;

DI float bf_lo(unsigned u) { return __builtin_bit_cast(float, u << 16); }
DI float bf_hi(unsigned u) { return __builtin_bit_cast(float, u & 0xffff0000u); }
DI unsigned pk2(float a, float b) {
  f32x2 p = {a, b};
  return __builtin_bit_cast(unsigned, __builtin_convertvector(p, bf16x2));
}
DI float rbf(float a) { return bf_lo(pk2(a, a)); }
template <int CTRL> DI float dpp_add(float v) {
  return v + __builtin_bit_cast(float, __builtin_amdgcn_update_dpp(0, __builtin_bit_cast(int, v), CTRL, 0xf, 0xf, true));
}
DI int fresh_lane() { int v = threadIdx.x & 63; asm volatile("" : "+v"(v)); return v; }
DI bf16x8 lds_b128(unsigned addr) { return *(LAS bf16x8*)(size_t)addr; }
DI s16x4 lds_tr(unsigned addr) { return __builtin_amdgcn_ds_read_tr16_b64_v4i16((LAS s16x4*)(size_t)addr); }
DI bf16x8 join(s16x4 lo, s16x4 hi) {
  return __builtin_bit_cast(bf16x8, __builtin_shufflevector(lo, hi, 0, 1, 2, 3, 4, 5, 6, 7));
}
DI f32x4 mfma(bf16x8 a, bf16x8 b, f32x4 c) { return __builtin_amdgcn_mfma_f32_16x16x32_bf16(a, b, c, 0, 0, 0); }
DI float pos(float v) { const int b = __builtin_bit_cast(int, v); return __builtin_bit_cast(float, b > 0 ? b : 0); }
DI float neg(float v) { const int b = __builtin_bit_cast(int, v); return __builtin_bit_cast(float, b < 0 ? b : 0); }
DI f32x2 relu2(float a, float b) { const f32x2 p = {pos(a), pos(b)}; return p * p; }
DI unsigned phi2(float a, float b) {
  const f32x2 m = (f32x2){a, b} * 1.44269504088896f;
  const f32x2 e = {__builtin_amdgcn_exp2f(neg(m[0])), __builtin_amdgcn_exp2f(neg(m[1]))};
  const f32x2 r = ((f32x2){pos(a), pos(b)} + 1e-6f) + e;
  return __builtin_bit_cast(unsigned, __builtin_convertvector(r, bf16x2));
}
DI unsigned pk2v(f32x2 p) { return __builtin_bit_cast(unsigned, __builtin_convertvector(p, bf16x2)); }
DI unsigned ld_flag(const unsigned* p) { return __hip_atomic_load((GAS unsigned*)p, __ATOMIC_RELAXED, __HIP_MEMORY_SCOPE_AGENT); }
DI void st_flag(unsigned* p, unsigned v) { __hip_atomic_store((GAS unsigned*)p, v, __ATOMIC_RELAXED, __HIP_MEMORY_SCOPE_AGENT); }
DI void wait_flags(const unsigned* base, int count, unsigned tag, int lane, unsigned first) {
  unsigned v = first;
#pragma unroll 1
  for (unsigned spins = 0; spins < SPIN_CAP; ++spins) {
    if (__all(lane >= count || v == tag)) break;
    __builtin_amdgcn_s_sleep(2);
    if (lane < count) v = ld_flag(base + lane);
  }
  asm volatile("" ::: "memory");
}
DI f32x4 unpk_lo(u32x4 v) { return (f32x4){bf_lo(v[0]), bf_hi(v[0]), bf_lo(v[1]), bf_hi(v[1])}; }
DI f32x4 unpk_hi(u32x4 v) { return (f32x4){bf_lo(v[2]), bf_hi(v[2]), bf_lo(v[3]), bf_hi(v[3])}; }

DI void dma(unsigned voff, const char* sbase, unsigned m0v) {
  asm volatile("s_mov_b32 m0, %1\n\ts_nop 0\n\tglobal_load_lds_dwordx4 %0, %2" ::"v"(voff), "s"(m0v), "s"(sbase) : "memory");
}
template <int B, int P> DI void piece(const unsigned (&voff)[5], const char* sbase, unsigned m) {
  asm volatile("s_add_i32 m0, %1, %3\n\ts_nop 0\n\tglobal_load_lds_dwordx4 %0, %2 offset:%4"
               ::"v"(voff[P]), "s"(m), "s"(sbase), "n"(B * 4096 + P * 1024 - B * 128), "n"(B * 128) : "memory", "scc");
}
DI void touch(const char* sbase, unsigned voff, unsigned& dummy) { asm volatile("global_load_dword %0, %1, %2" : "+v"(dummy) : "v"(voff), "s"(sbase) : "memory"); }

template <int BOFF0>
DI void kstep9(f32x4 (&acc)[9][4], bf16x8 (&A)[9], const bf16x8 (&Bc)[4], bf16x8 (&Bn)[4], unsigned ab, unsigned abn, unsigned bn) {
  s16x4 lx, ly;
  constexpr int BTS = 16 * ACT_STRIDE;
#define MM(blk, j) acc[blk][j] = mfma(A[blk], Bc[j], acc[blk][j])
#define RL(v, base, blk) v = lds_tr((base) + (blk) * 4096)
#define RH(v, base, blk) A[blk] = join(v, lds_tr((base) + (blk) * 4096 + 512))
  RL(lx, ab, 3); SCHED(); MM(0, 0); SCHED(); RH(lx, ab, 3); SCHED(); MM(0, 1); SCHED(); MM(0, 2); MM(0, 3); SCHED();
  MM(1, 0); SCHED(); Bn[0] = lds_b128(bn + BOFF0); SCHED(); MM(1, 1); SCHED();
  Bn[1] = lds_b128(bn + BOFF0 + BTS); SCHED(); MM(1, 2); MM(1, 3); SCHED();
  WAIT_LGKM(2);
  BAR();
  RL(lx, ab, 4); SCHED(); MM(2, 0); SCHED(); RH(lx, ab, 4); SCHED(); MM(2, 1); SCHED();
  RL(ly, ab, 5); SCHED(); MM(2, 2); SCHED(); RH(ly, ab, 5); SCHED(); MM(2, 3); SCHED();
  RL(lx, ab, 6); SCHED(); MM(3, 0); SCHED(); RH(lx, ab, 6); SCHED(); MM(3, 1); SCHED(); MM(3, 2); MM(3, 3); SCHED();
  RL(lx, ab, 7); SCHED(); MM(4, 0); SCHED(); RH(lx, ab, 7); SCHED(); MM(4, 1); SCHED(); MM(4, 2); MM(4, 3); SCHED();
  RL(lx, ab, 8); SCHED(); MM(5, 0); SCHED(); RH(lx, ab, 8); SCHED(); MM(5, 1); SCHED(); MM(5, 2); MM(5, 3); SCHED();
  MM(6, 0); SCHED(); Bn[2] = lds_b128(bn + BOFF0 + 2 * BTS); SCHED(); MM(6, 1); SCHED();
  Bn[3] = lds_b128(bn + BOFF0 + 3 * BTS); SCHED(); MM(6, 2); MM(6, 3); SCHED();
  WAIT_LGKM(2);
  BAR();
  RL(lx, abn, 0); SCHED(); MM(7, 0); SCHED(); RH(lx, abn, 0); SCHED(); MM(7, 1); SCHED();
  RL(ly, abn, 1); SCHED(); MM(7, 2); SCHED(); RH(ly, abn, 1); SCHED(); MM(7, 3); SCHED();
  RL(lx, abn, 2); SCHED(); MM(8, 0); SCHED(); RH(lx, abn, 2); SCHED(); MM(8, 1); SCHED(); MM(8, 2); MM(8, 3); SCHED();
}
template <int BOFF0>
DI void kstep4(f32x4 (&acc)[4][4], bf16x8 (&A)[4], const bf16x8 (&Bc)[4], bf16x8 (&Bn)[4], unsigned ab, unsigned abn, unsigned d3, unsigned bn) {
  s16x4 lx, ly;
  constexpr int BTS = 16 * ACT_STRIDE;
#define QL(v, base, off) v = lds_tr((base) + (off))
#define QH(v, i, base, off) A[i] = join(v, lds_tr((base) + (off) + 576))
  const unsigned a3 = ab + d3;
  QL(lx, a3, 0); SCHED(); MM(0, 0); SCHED(); QH(lx, 3, a3, 0); SCHED(); MM(0, 1); SCHED();
  Bn[0] = lds_b128(bn + BOFF0); SCHED(); MM(0, 2); SCHED(); Bn[1] = lds_b128(bn + BOFF0 + BTS); SCHED(); MM(0, 3); SCHED();
  Bn[2] = lds_b128(bn + BOFF0 + 2 * BTS); SCHED(); MM(1, 0); SCHED(); Bn[3] = lds_b128(bn + BOFF0 + 3 * BTS); SCHED();
  MM(1, 1); MM(1, 2); MM(1, 3); SCHED();
  WAIT_LGKM(4);
  BAR();
  QL(lx, abn, 0); SCHED(); MM(2, 0); SCHED(); QH(lx, 0, abn, 0); SCHED(); MM(2, 1); SCHED();
  QL(ly, abn, 5120); SCHED(); MM(2, 2); SCHED(); QH(ly, 1, abn, 5120); SCHED(); MM(2, 3); SCHED();
  QL(lx, abn, 10240); SCHED(); MM(3, 0); SCHED(); QH(lx, 2, abn, 10240); SCHED(); MM(3, 1); MM(3, 2); MM(3, 3); SCHED();
}
#undef MM

}

__global__ __launch_bounds__(512, 2) void fused_block_kernel(
    const unsigned short* __restrict__ x, const char* __restrict__ wqkv, const char* __restrict__ wo,
    const char* __restrict__ w1, const unsigned short* __restrict__ b1, const char* __restrict__ w2,
    const unsigned short* __restrict__ b2, unsigned short* __restrict__ out, char* __restrict__ ws) {
  extern __shared__ __attribute__((aligned(16))) char lds[];
  const int tid = threadIdx.x, lane = tid & 63;
  const int wid = __builtin_amdgcn_readfirstlane(tid >> 6);
  constexpr int NGX = MAP_NGX, PFN = 32 / NGX;
  const int xj = blockIdx.x & 7, xi = blockIdx.x >> 3;
  const int g = NGX * (xj / NGX) + xi % NGX, pfr = xi / NGX, t = (xj % NGX) * PFN + pfr;
  const unsigned LB = (unsigned)(size_t)lds;
  const __amdgpu_buffer_rsrc_t wsr = __builtin_amdgcn_make_buffer_rsrc(ws, 0, WS_END, 0x00020000);

  unsigned long long pc0;
  asm volatile("s_getpc_b64 %0" : "=s"(pc0));
  STAMP_DECL
  STAMP(0); STAMP_RT(20);
  const int lr = lane >> 3, lc = lane & 7;
  const int l = wid - 4;
  const int cE = lc ^ (((lr >> 1) & 1) << 1), cO = cE ^ 4;
  unsigned voff[5];
  const unsigned m0r = LB + RING;
  const char* baseQ = wqkv + g * 144 + l * 1152;
  auto issueQ = [&](const char* sq, unsigned mq) {
    dma(voff[0], sq, mq); dma(voff[1], sq, mq + 1024); dma(voff[2], sq, mq + 2048); dma(voff[3], sq, mq + 3072); dma(voff[4], sq, mq + 4096);
  };
  unsigned dummy = 0;
  unsigned epoch;
  if (wid < 4) {
    u32x4 xr[2][9];
    const char* xp = (const char*)x + (size_t)(t * 64 + wid * 16) * 1152;
    const unsigned xo = lr * 1152 + lc * 16;
#pragma unroll
    for (int h = 0; h < 2; ++h)
#pragma unroll
      for (int i = 0; i < 9; ++i)
        asm volatile("global_load_dwordx4 %0, %1, %2 offset:%3 " XPOL : "=v"(xr[h][i]) : "v"(xo + h * 8 * 1152), "s"(xp), "n"(i * 128) : "memory");
    asm volatile("global_load_dword %0, %1, %2 sc1" : "=v"(epoch) : "v"(0u), "s"(ws) : "memory");
#pragma unroll
    for (int h = 0; h < 2; ++h) {
      if (h == 0) WAIT_VM(10); else WAIT_VM(0);
      SCHED();
#pragma unroll
      for (int i = 0; i < 9; ++i) asm volatile("" : "+v"(xr[h][i]));
      if (h == 0) STAMP(1);
      const bf16x2 ones = __builtin_bit_cast(bf16x2, 0x3f803f80u);
      float s0 = 0.f, s1 = 0.f, q0 = 0.f, q1 = 0.f;
#pragma unroll
      for (int i = 0; i < 9; ++i) {
        const unsigned u0 = xr[h][i][0], u1 = xr[h][i][1], u2 = xr[h][i][2], u3 = xr[h][i][3];
        const bf16x2 a = __builtin_bit_cast(bf16x2, u0), b = __builtin_bit_cast(bf16x2, u1);
        const bf16x2 c = __builtin_bit_cast(bf16x2, u2), d = __builtin_bit_cast(bf16x2, u3);
        s0 = __builtin_amdgcn_fdot2_f32_bf16(a, ones, s0, false); s1 = __builtin_amdgcn_fdot2_f32_bf16(b, ones, s1, false);
        q0 = __builtin_amdgcn_fdot2_f32_bf16(a, a, q0, false); q1 = __builtin_amdgcn_fdot2_f32_bf16(b, b, q1, false);
        s0 = __builtin_amdgcn_fdot2_f32_bf16(c, ones, s0, false); s1 = __builtin_amdgcn_fdot2_f32_bf16(d, ones, s1, false);
        q0 = __builtin_amdgcn_fdot2_f32_bf16(c, c, q0, false); q1 = __builtin_amdgcn_fdot2_f32_bf16(d, d, q1, false);
      }
      float s = s0 + s1, q = q0 + q1;
      s = dpp_add<0xB1>(s); q = dpp_add<0xB1>(q);
      s = dpp_add<0x4E>(s); q = dpp_add<0x4E>(q);
      s = dpp_add<0x141>(s); q = dpp_add<0x141>(q);
      const float mean = s * (1.f / 576.f);
      const float var = fmaxf(q * (1.f / 576.f) - mean * mean, 0.f);
      const float mb = rbf(mean);
      const float sd = rbf(__builtin_amdgcn_sqrtf(rbf(rbf(var) + 1e-5f)));
      const float rs = __builtin_amdgcn_rcpf(sd), nb = -mb * rs;
      const unsigned wa = LB + (wid * 16 + h * 8 + lr) * ACT_STRIDE + lc * 16;
#pragma unroll
      for (int i = 0; i < 9; ++i) {
        u32x4 o;
#pragma unroll
        for (int e = 0; e < 4; ++e) o[e] = pk2(fmaf(bf_lo(xr[h][i][e]), rs, nb), fmaf(bf_hi(xr[h][i][e]), rs, nb));
        *(LAS u32x4*)(size_t)(wa + i * 128) = o;
      }
    }
  } else {
    if (wid == 7) {
      const unsigned i = (unsigned)lane * 32u + (unsigned)xi;
      touch((const char*)pc0, (i < CODE_PF_BYTES / 128 ? i : 0u) * 128u, dummy);
    }
    asm volatile("global_load_dword %0, %1, %2 sc1" : "=v"(epoch) : "v"(0u), "s"(ws) : "memory");
    if (wid == 7) {
      constexpr int RW = 576 / PFN, NQ = (RW + 7) / 8;
      const char* pq_ = wqkv + (size_t)(RW * pfr) * 3456 + ((g * 144) & ~127);
      const int c6 = lc < 5 ? lc : 5;
#pragma unroll
      for (int n = 0; n < NQ; ++n) {
        const int r = 8 * n + lr < RW ? 8 * n + lr : RW - 1;
        touch(pq_, r * 3456 + (c6 >> 1) * 1152 + (c6 & 1) * 128, dummy);
      }
      WAIT_VM(NQ);
    } else {
#pragma unroll
      for (int p = 0; p < 5; ++p) {
        int i = 64 * p + lane; i = i < 288 ? i : 287;
        const int r = (i * 7282) >> 16;
        voff[p] = r * 3456 + (i - 9 * r) * 16;
      }
      issueQ(baseQ, m0r + l * 5120);
      WAIT_VM(5);
    }
    SCHED();
    STAMP(1);
  }
  asm volatile("" : "+v"(epoch));
  const unsigned tag = __builtin_amdgcn_readfirstlane(epoch) + 1u;
  STAMP(2);

  if (wid >= 4) {
#define LPRIO 2
#define CPRIO 3
    __builtin_amdgcn_s_setprio(LPRIO);
    const char* base1 = w1 + g * 1152;
    const char* base2 = w2 + (size_t)(g * 576) * 1152;
    const char* baseO = wo + (size_t)(g * 72) * 1152;
    auto setvoff = [&](unsigned ldw, int rowsel) {
#pragma unroll
      for (int p = 0; p < 4; ++p) voff[p] = (rowsel * 8 * p + lr) * ldw + ((p & 1) ? cO : cE) * 16;
    };
    using I1 = std::integral_constant<int, 1>;
    WAIT_LGKM(0);
    if (l < 3) {
      auto prog = [&](auto LC) {
        constexpr int L = decltype(LC)::value;
        auto issue8 = [&](const char* sb, unsigned m) {
          if constexpr (L == 0) { piece<8, 0>(voff, sb, m); piece<8, 3>(voff, sb, m); }
          if constexpr (L == 1) piece<8, 1>(voff, sb, m);
          if constexpr (L == 2) piece<8, 2>(voff, sb, m);
        };
        auto issueLo = [&](const char* sb, unsigned m) {
          if constexpr (L == 0) { piece<0, 2>(voff, sb, m); piece<1, 1>(voff, sb, m); piece<2, 0>(voff, sb, m); piece<2, 3>(voff, sb, m); piece<3, 2>(voff, sb, m); }
          if constexpr (L == 1) { piece<0, 0>(voff, sb, m); piece<0, 3>(voff, sb, m); piece<1, 2>(voff, sb, m); piece<2, 1>(voff, sb, m); piece<3, 0>(voff, sb, m); piece<3, 3>(voff, sb, m); }
          if constexpr (L == 2) { piece<0, 1>(voff, sb, m); piece<1, 0>(voff, sb, m); piece<1, 3>(voff, sb, m); piece<2, 2>(voff, sb, m); piece<3, 1>(voff, sb, m); }
        };
        auto issueHi = [&](const char* sb, unsigned m) {
          if constexpr (L == 0) { piece<4, 1>(voff, sb, m); piece<5, 0>(voff, sb, m); piece<5, 3>(voff, sb, m); piece<6, 2>(voff, sb, m); piece<7, 1>(voff, sb, m); }
          if constexpr (L == 1) { piece<4, 2>(voff, sb, m); piece<5, 1>(voff, sb, m); piece<6, 0>(voff, sb, m); piece<6, 3>(voff, sb, m); piece<7, 2>(voff, sb, m); }
          if constexpr (L == 2) { piece<4, 0>(voff, sb, m); piece<4, 3>(voff, sb, m); piece<5, 2>(voff, sb, m); piece<6, 1>(voff, sb, m); piece<7, 0>(voff, sb, m); piece<7, 3>(voff, sb, m); }
        };
        constexpr int WX = L == 0 ? 10 : 11;
        const unsigned mA = m0r, mB = m0r + KSLOT;
        constexpr int RW1 = 576 / PFN, NT = (RW1 * 9 + 191) / 192;
        {
          const char* sq = baseQ + 32 * 3456;
          const unsigned mq = m0r + L * 5120;
          issueQ(sq, mq + 16384); issueQ(sq + 32 * 3456, mq + KSLOT);
          sq += 64 * 3456;
          {
            const char* p1 = w1 + (size_t)(RW1 * pfr) * 9216 + g * 1152;
#pragma unroll
            for (int n = 0; n < NT; ++n) {
              const int i0 = (3 * n + L) * 64 + lane, i = i0 < RW1 * 9 ? i0 : 0;
              const int r = (i * 7282) >> 16;
              touch(p1, r * 9216 + (i - 9 * r) * 128, dummy);
            }
          }
          STAMP(15);
          WAIT_VM(10 + NT);
          STAMP(16);
          BAR();
          STAMP(3);
          unsigned m = mq + KSLOT + 16384, dA = (unsigned)-(KSLOT + 16384), dB = 16384;
#pragma unroll 1
          for (int j = 3; j < 18; ++j) {
            issueQ(sq, m);
            sq += 32 * 3456;
            m += dA;
            const unsigned nA = dA == 16384 ? ((m - mq) >= (unsigned)KSLOT ? (unsigned)-(KSLOT + 16384) : (unsigned)(KSLOT - 16384)) : 16384u;
            dA = nA;
            (void)dB;
            if (j < 5) WAIT_VM(10 + NT); else WAIT_VM(10);
            BAR();
          }
        }
        setvoff(9216, 1);
        issueLo(base1, mB); WAIT_VM(10); BAR();
        issueHi(base1, mB); issue8(base1, mB); WAIT_VM(12); BAR();
        issueLo(base1 + 32 * 9216, mA); BAR(); BAR();
        WAIT_VM(12); BAR();
        {
          const char* s1 = base1 + 32 * 9216;
          const char* s2 = s1 + 32 * 9216;
          unsigned step = 32 * 9216, m1 = mA, m2 = mB;
#pragma unroll 1
          for (int kk = 0; kk < 37; ++kk) {
            issueHi(s1, m1);
            if (__builtin_expect(kk == 18, 0)) BAR();
            WAIT_VM(WX); BAR();
            issue8(s1, m1);
            if (__builtin_expect((0x1000010000ull >> kk) & 1, 0)) {
              if (kk == 16) { setvoff(1152, 1); s2 = base2; step = 32 * 1152; }
              else setvoff(1152, 0);
            }
            issueLo(s2, m2); WAIT_VM(12); BAR();
            s1 = s2;
            s2 = kk == 33 ? baseO : s2 + step;
            const unsigned tm = m1; m1 = m2; m2 = tm;
          }
          issueHi(s1, m1); WAIT_VM(WX); BAR();
          issue8(s1, m1); WAIT_VM(6); BAR();
          WAIT_VM(0); BAR(); BAR();
        }
      };
      if (l == 0) prog(std::integral_constant<int, 0>{});
      else if (l == 1) prog(std::integral_constant<int, 1>{});
      else prog(std::integral_constant<int, 2>{});
    } else {
      auto bars = [&](int n) {
#pragma unroll 1
        for (int i = 0; i < n; ++i) BAR();
      };
      const int lane = fresh_lane();
      constexpr int RW = 576 / PFN;
      BAR();
      STAMP(3);
      bars(17 + 1);
      {
        const char* p2 = base2 + (size_t)(RW * pfr) * 1152;
#pragma unroll
        for (int n = 0; n < (RW * 9 + 63) / 64; ++n) {
          const int i = lane + 64 * n < RW * 9 ? lane + 64 * n : 0;
          touch(p2, i * 128, dummy);
        }
        constexpr int WOL = (648 + PFN - 1) / PFN;
        static_assert(WOL <= 64, "prefetch steps");
        int i = pfr * WOL + lane; i = (lane < WOL && i < 648) ? i : 0;
        touch(baseO, i * 128, dummy);
      }
      bars(2);
      bars(13);
      if (lane == 0) st_flag((unsigned*)(ws + WS_FLAG1) + g * 32 + t, tag);
      bars(7);
      unsigned fl = lane < 32 ? ld_flag((const unsigned*)(ws + WS_FLAG1) + g * 32 + lane) : tag;
      bars(3);
      STAMP(16);
      wait_flags((const unsigned*)(ws + WS_FLAG1) + g * 32, 32, tag, lane, fl);
      STAMP(17);
      {
        u32x4 hv[16];
        const unsigned po = WS_P + (g * 32 + (lane >> 5)) * 16384 + t * 512 + (lane & 31) * 16;
#pragma unroll
        for (int n = 0; n < 16; ++n)
          hv[n] = __builtin_amdgcn_raw_buffer_load_b128(wsr, po, n * 32768, AUX_SC1);
        SCHED();
        bars(5);
        f32x4 a = __builtin_bit_cast(f32x4, hv[0]), b = __builtin_bit_cast(f32x4, hv[1]);
#pragma unroll
        for (int n = 2; n < 16; n += 2) { a += __builtin_bit_cast(f32x4, hv[n]); b += __builtin_bit_cast(f32x4, hv[n + 1]); }
        a += b;
        SCHED();
        BAR();
        const int hh = lane >> 5, li = 32 * (t & 1) + (lane & 31), tile = t >> 1;
        float s0 = hh ? a[2] : a[0], s1 = hh ? a[3] : a[1], o0 = hh ? a[0] : a[2], o1 = hh ? a[1] : a[3];
        const unsigned x0 = (unsigned)__builtin_amdgcn_permlane32_swap(__builtin_bit_cast(unsigned, o0), __builtin_bit_cast(unsigned, o0), false, false)[hh ? 0 : 1];
        const unsigned x1 = (unsigned)__builtin_amdgcn_permlane32_swap(__builtin_bit_cast(unsigned, o1), __builtin_bit_cast(unsigned, o1), false, false)[hh ? 0 : 1];
        s0 += __builtin_bit_cast(float, x0); s1 += __builtin_bit_cast(float, x1);
        const int di = (int)((0x4433322221111000ull >> (4 * tile)) & 15u), ej = (int)((0x4343243214210410ull >> (4 * tile)) & 15u);
        const int d = 16 * di + 4 * (li >> 4) + 2 * hh, e = 16 * ej + (li & 15);
        const int hd = (d * 57) >> 10, he = (e * 57) >> 10;
        const bool ok = d < 72 && (e == 72 || (e < 72 && hd == he));
        const int row = e == 72 ? 18 : e - 18 * he;
        __builtin_amdgcn_raw_buffer_store_b32(pk2(s0, s1), wsr, ok ? WS_S + g * 8192 + hd * 1280 + row * 64 + (d - 18 * hd) * 2 : OOB, 0, AUX_SC1);
      }
      bars(5);
      WAIT_VM(0);
      if (lane == 0) st_flag((unsigned*)(ws + WS_FLAG2) + g * 32 + t, tag);
      bars(2 + 1 + 7);
      fl = lane < 32 ? ld_flag((const unsigned*)(ws + WS_FLAG2) + g * 32 + lane) : tag;
      bars(3);
      STAMP(18);
      wait_flags((const unsigned*)(ws + WS_FLAG2) + g * 32, 32, tag, lane, fl);
      STAMP(19);
      {
        u32x4 iv[5];
#pragma unroll
        for (int n = 0; n < 5; ++n) iv[n] = __builtin_amdgcn_raw_buffer_load_b128(wsr, WS_S + g * 8192 + lane * 16, n * 1024, AUX_SC1);
        SCHED();
        bars(5);
        const int c = lane & 3;
#pragma unroll
        for (int n = 0; n < 5; ++n) {
          const int row = 16 * n + (lane >> 2);
          const bool live = row != 19 && row != 39 && row != 59 && row != 79 && c < 3;
          u32x4 v = iv[n];
          v[0] = live ? v[0] : 0u;
          v[1] = (live && c < 2) ? v[1] : 0u; v[2] = (live && c < 2) ? v[2] : 0u; v[3] = (live && c < 2) ? v[3] : 0u;
          *(LAS u32x4*)(size_t)(LB + SIMG + n * 1024 + lane * 16) = v;
        }
        WAIT_LGKM(0);
      }
      bars(27);
    }
    STAMP(8);
    WAIT_VM(0);
    asm volatile("" ::"v"(dummy));
  } else {
    const int w = wid;
    __builtin_amdgcn_s_setprio(CPRIO);
    const int g4 = lane >> 4, q = (lane & 15) >> 2, p = lane & 3;
    const int cf = (((q >> 1) & 1) << 1) | ((g4 & 1) << 2);
    unsigned a0 = LB + RING + (8 * g4 + q) * 128 + (((2 * w + (p >> 1)) ^ cf) * 16) + (p & 1) * 8;
    unsigned a1 = a0 + KSLOT;
    asm volatile("" : "+v"(a1));
    const unsigned b0 = LB + (lane & 15) * ACT_STRIDE + g4 * 16;
    bf16x8 B0[4], B1[4];
    f32x4 acc[9][4];
    {
      f32x4 qa[4][4];
      bf16x8 A[4];
#pragma unroll
      for (int i = 0; i < 4; ++i)
#pragma unroll
        for (int j = 0; j < 4; ++j) qa[i][j] = (f32x4){0.f, 0.f, 0.f, 0.f};
      STAMP(15);
      WAIT_LGKM(0);
      BAR();
      STAMP(3);
#pragma unroll
      for (int j = 0; j < 4; ++j) B0[j] = lds_b128(b0 + j * 16 * ACT_STRIDE);
      unsigned ac = LB + RING + (8 * g4 + q) * 144 + 32 * w + 8 * p, an = ac + KSLOT, bc = b0;
      const unsigned d3 = (w < 3 ? w : 0) * 5120 + 128 - 32 * w;
      A[0] = join(lds_tr(ac), lds_tr(ac + 576));
      A[1] = join(lds_tr(ac + 5120), lds_tr(ac + 5120 + 576));
      A[2] = join(lds_tr(ac + 10240), lds_tr(ac + 10240 + 576));
#pragma unroll 1
      for (int it = 0; it < 9; ++it) {
        kstep4<64>(qa, A, B0, B1, ac, ac + 16384, d3, bc);
        kstep4<128>(qa, A, B1, B0, ac + 16384, an, d3, bc);
        const unsigned tmp = ac; ac = an; an = tmp;
        bc += 128;
      }
      STAMP(4);
      const int lane = fresh_lane(), g4 = lane >> 4, q = (lane & 15) >> 2, p = lane & 3;
      u32x2 bb[9];
#pragma unroll
      for (int blk = 0; blk < 9; ++blk) bb[blk] = *(const u32x2*)(b1 + g * 576 + 64 * blk + 16 * w + 4 * g4);
      {
        const unsigned tk = (lane & 15), col = (16 * w + 4 * g4) * 2;
        const unsigned aq = LB + PQ + tk * 144 + col, ak = LB + KTMP + tk * 144 + col, av = LB + VTMP + tk * 152 + col;
        constexpr unsigned DROP = 0x40000;
        const unsigned ar = w == 3 ? (g4 == 0 ? LB + VTMP + tk * 152 + 144 : DROP)
                                   : g4 >= 2 ? DROP : (w == 0 ? LB + PQ + tk * 144 : w == 1 ? LB + KTMP + tk * 144 : LB + VTMP + tk * 152) + 128 + g4 * 8;
        const unsigned rs = w >= 2 ? 16 * 152 : 16 * 144;
        u32x2 oq[4], ok[4], ov[4], orm[4];
#pragma unroll
        for (int tt = 0; tt < 4; ++tt) {
          ov[tt] = (u32x2){pk2(qa[2][tt][0], qa[2][tt][1]), pk2(qa[2][tt][2], qa[2][tt][3])};
          ok[tt] = (u32x2){phi2(qa[1][tt][0], qa[1][tt][1]), phi2(qa[1][tt][2], qa[1][tt][3])};
        }
        SCHED();
#pragma unroll
        for (int tt = 0; tt < 4; ++tt) {
          *(LAS u32x2*)(size_t)(av + tt * 16 * 152) = ov[tt];
          *(LAS u32x2*)(size_t)(ak + tt * 16 * 144) = ok[tt];
        }
        if (w < 2) {
#pragma unroll
          for (int tt = 0; tt < 4; ++tt) orm[tt] = (u32x2){phi2(qa[3][tt][0], qa[3][tt][1]), phi2(qa[3][tt][2], qa[3][tt][3])};
        } else {
#pragma unroll
          for (int tt = 0; tt < 4; ++tt) {
            orm[tt] = (u32x2){pk2(qa[3][tt][0], qa[3][tt][1]), pk2(qa[3][tt][2], qa[3][tt][3])};
            if (w == 3) orm[tt] = (u32x2){0x00003f80u, 0u};
          }
        }
#pragma unroll
        for (int tt = 0; tt < 4; ++tt) oq[tt] = (u32x2){phi2(qa[0][tt][0], qa[0][tt][1]), phi2(qa[0][tt][2], qa[0][tt][3])};
        SCHED();
#pragma unroll
        for (int tt = 0; tt < 4; ++tt) {
          *(LAS u32x2*)(size_t)(ar + tt * rs) = orm[tt];
          *(LAS u32x2*)(size_t)(aq + tt * 16 * 144) = oq[tt];
        }
      }
      WAIT_LGKM(0);
      BAR();
      const unsigned ti = (unsigned)(0x4433322221111000ull >> (16 * w)) & 0xffffu;
      const unsigned tj = (unsigned)(0x4343243214210410ull >> (16 * w)) & 0xffffu;
      const unsigned kr = LB + KTMP + (8 * g4 + q) * 144 + p * 8, vr = LB + VTMP + (8 * g4 + q) * 152 + p * 8;
#pragma unroll
      for (int n = 0; n < 4; ++n) {
        const unsigned di = (ti >> (4 * n)) & 15u, ej = (tj >> (4 * n)) & 15u;
        f32x4 d = {0.f, 0.f, 0.f, 0.f};
#pragma unroll
        for (int ks = 0; ks < 2; ++ks) {
          const unsigned ka = kr + ks * 32 * 144 + di * 32, va = vr + ks * 32 * 152 + ej * 32;
          const bf16x8 fa = join(lds_tr(ka), lds_tr(ka + 4 * 144));
          const bf16x8 fb = join(lds_tr(va), lds_tr(va + 4 * 152));
          d = mfma(fa, fb, d);
        }
        __builtin_amdgcn_raw_buffer_store_b128(__builtin_bit_cast(u32x4, d), wsr,
                                               WS_P + (g * 32 + t) * 16384 + (4 * w + n) * 1024 + lane * 16, 0, AUX_SC1);
      }
#pragma unroll
      for (int i = 0; i < 9; ++i) {
        const f32x4 bv = {bf_lo(bb[i][0]), bf_hi(bb[i][0]), bf_lo(bb[i][1]), bf_hi(bb[i][1])};
#pragma unroll
        for (int j = 0; j < 4; ++j) acc[i][j] = bv;
      }
      WAIT_LGKM(0);
      BAR();
      STAMP(5);
    }
    bf16x8 A[9];
    {
#pragma unroll
      for (int j = 0; j < 4; ++j) B0[j] = lds_b128(b0 + j * 16 * ACT_STRIDE);
      A[0] = join(lds_tr(a1), lds_tr(a1 + 512));
      A[1] = join(lds_tr(a1 + 4096), lds_tr(a1 + 4096 + 512));
      A[2] = join(lds_tr(a1 + 8192), lds_tr(a1 + 8192 + 512));
      unsigned bc = b0;
#pragma unroll 1
      for (int it = 0; it < 9; ++it) {
        if (__builtin_expect(it == 3, 0)) WAIT_VM(0);
        kstep9<64>(acc, A, B0, B1, a1, a0, bc);
        kstep9<128>(acc, A, B1, B0, a0, a1, bc);
        bc += 128;
      }
      STAMP(6);
      const int lane = fresh_lane(), g4 = lane >> 4;
      const unsigned ha = LB + (lane & 15) * ACT_STRIDE + (16 * w + 8 * (g4 >> 1)) * 2;
      u32x4 hq[18];
#pragma unroll
      for (int n = 0; n < 18; ++n) {
        const f32x4 u = n < 16 ? acc[2 * (n & 3)][n >> 2] : acc[8][2 * (n - 16)];
        const f32x4 v = n < 16 ? acc[2 * (n & 3) + 1][n >> 2] : acc[8][2 * (n - 16) + 1];
        const auto s0 = __builtin_amdgcn_permlane16_swap(pk2v(relu2(u[0], u[1])), pk2v(relu2(v[0], v[1])), false, false);
        const auto s1 = __builtin_amdgcn_permlane16_swap(pk2v(relu2(u[2], u[3])), pk2v(relu2(v[2], v[3])), false, false);
        hq[n] = (u32x4){(unsigned)s0[0], (unsigned)s1[0], (unsigned)s0[1], (unsigned)s1[1]};
        if (n >= 2) {
          SCHED();
          const int m = n - 2;
          *(LAS u32x4*)(size_t)(ha + (m >> 2) * 16 * ACT_STRIDE + (128 * (m & 3) + 64 * (g4 & 1)) * 2) = hq[m];
          SCHED();
        }
      }
      *(LAS u32x4*)(size_t)(ha + (g4 & 1) * 16 * ACT_STRIDE + 512 * 2) = hq[16];
      *(LAS u32x4*)(size_t)(ha + (2 + (g4 & 1)) * 16 * ACT_STRIDE + 512 * 2) = hq[17];
      WAIT_LGKM(0);
      BAR();
      STAMP(7);
    }
    {
#pragma unroll
      for (int j = 0; j < 4; ++j) B0[j] = lds_b128(b0 + j * 16 * ACT_STRIDE);
#pragma unroll
      for (int i = 0; i < 9; ++i)
#pragma unroll
        for (int j = 0; j < 4; ++j) acc[i][j] = (f32x4){0.f, 0.f, 0.f, 0.f};
      unsigned bc = b0;
      kstep9<64>(acc, A, B0, B1, a1, a0, bc);
#pragma unroll 1
      for (int it = 0; it < 10; ++it) {
        if (__builtin_expect(it == 6, 0)) {
          const int lane = fresh_lane(), g4 = lane >> 4, r16 = lane & 15;
          const unsigned sa = LB + SIMG + w * 1280 + g4 * 16;
          const bf16x8 ka0 = lds_b128(sa + r16 * 64), ka1 = lds_b128(sa + (r16 < 3 ? 16 + r16 : 19) * 64);
          const unsigned pa = LB + PQ + r16 * PQ_STRIDE + w * 36 + g4 * 16;
          const unsigned oa = LB + r16 * ACT_STRIDE + w * 36 + g4 * 8;
          const unsigned la = g4 == 0 ? oa + 32 : LB + r16 * ACT_STRIDE + 144 + w * 12 + (g4 - 1) * 4;
          u32x4 pb[4];
#pragma unroll
          for (int tt = 0; tt < 4; ++tt)
#pragma unroll
            for (int m = 0; m < 4; ++m) pb[tt][m] = *(LAS unsigned*)(size_t)(pa + tt * 16 * PQ_STRIDE + 4 * m);
#pragma unroll
          for (int tt = 0; tt < 4; ++tt) {
            const bf16x8 b = __builtin_bit_cast(bf16x8, pb[tt]);
            const f32x4 n0 = mfma(ka0, b, (f32x4){0.f, 0.f, 0.f, 0.f}), n1 = mfma(ka1, b, (f32x4){0.f, 0.f, 0.f, 0.f});
            const float d2 = n1[2];
            const unsigned du = __builtin_bit_cast(unsigned, d2);
            const unsigned y = (unsigned)__builtin_amdgcn_permlane32_swap(du, du, false, false)[0];
            const unsigned z = (unsigned)__builtin_amdgcn_permlane16_swap(y, y, false, false)[0];
            const float r = __builtin_amdgcn_rcpf(__builtin_bit_cast(float, z) + 1e-6f);
            const f32x2 p0 = (f32x2){n0[0], n0[1]} * r, p1 = (f32x2){n0[2], n0[3]} * r, p2 = (f32x2){n1[0], n1[1]} * r;
            *(LAS unsigned*)(size_t)(oa + tt * 16 * ACT_STRIDE) = pk2v(p0);
            *(LAS unsigned*)(size_t)(oa + tt * 16 * ACT_STRIDE + 4) = pk2v(p1);
            *(LAS unsigned*)(size_t)(la + tt * 16 * ACT_STRIDE) = g4 == 0 ? pk2v(p2) : 0u;
          }
        }
        kstep9<128>(acc, A, B1, B0, a0, a1, bc);
        kstep9<192>(acc, A, B0, B1, a1, a0, bc);
        bc += it == 7 ? 128 - 18 * 64 : 128;
      }
    }
    STAMP(8);
    {
      const int lane = fresh_lane(), g4 = lane >> 4;
      const int hf = (lane >> 3) & 1, l32 = g4 * 8 + (lane & 7);
#pragma unroll
      for (int tq = 0; tq < 4; ++tq) {
        const int tt = tq;
        const bool own = 2 * tt + hf == g;
        const unsigned sb = own ? OOB : WS_SLAB + ((t * 8 + 2 * tt + hf) * 8 + g) * 9216;
        u32x4 o[4];
#pragma unroll
        for (int j = 0; j < 4; ++j) {
          o[j][0] = pk2(acc[2 * j][tt][0], acc[2 * j][tt][1]); o[j][1] = pk2(acc[2 * j][tt][2], acc[2 * j][tt][3]);
          o[j][2] = pk2(acc[2 * j + 1][tt][0], acc[2 * j + 1][tt][1]); o[j][3] = pk2(acc[2 * j + 1][tt][2], acc[2 * j + 1][tt][3]);
        }
        const u32x2 o2 = {pk2(acc[8][tt][0], acc[8][tt][1]), pk2(acc[8][tt][2], acc[8][tt][3])};
#pragma unroll
        for (int j = 0; j < 4; ++j) __builtin_amdgcn_raw_buffer_store_b128(o[j], wsr, sb + ((w * 4 + j) * 32 + l32) * 16, 0, AUX_SC1);
        __builtin_amdgcn_raw_buffer_store_b64(o2, wsr, sb + 8192 + (w * 32 + l32) * 8, 0, AUX_SC1);
        if (tt == (g >> 1)) {
          const unsigned fa = own ? LB + l32 * 16 + w * 2048 : 0x40000u;
#pragma unroll
          for (int j = 0; j < 4; ++j) *(LAS u32x4*)(size_t)(fa + j * 512) = o[j];
          *(LAS u32x2*)(size_t)(own ? LB + 8192 + (w * 32 + l32) * 8 : 0x40000u) = o2;
        }
      }
    }
    STAMP(9);
    WAIT_VM(0);
    STAMP(10);
  }
  {
    int tid = threadIdx.x; asm volatile("" : "+v"(tid));
    const int lane = tid & 63;
    const int tk = tid & 7, cg = (tid >> 3) & 3;
    const int ca = 128 * ((tid >> 5) & 3) + 16 * (tid >> 7) + 4 * cg, cr = 512 + 16 * ((tid >> 5) & 3) + 4 * cg;
    const size_t row = (size_t)(t * 64 + g * 8 + tk) * DM;
    const u32x2 xa = *(const u32x2*)(x + row + ca), xb = *(const u32x2*)(x + row + ca + 64);
    const u32x2 ba = *(const u32x2*)(b2 + ca), bb = *(const u32x2*)(b2 + ca + 64);
    u32x2 xc = xa, bc = ba;
    if (wid < 2) { xc = *(const u32x2*)(x + row + cr); bc = *(const u32x2*)(b2 + cr); }
    WAIT_LGKM(0);
    BAR();
    if (tid == 0) st_flag((unsigned*)(ws + WS_FLAG3) + t * 32 + g, tag);
    const u32x4 own = *(LAS u32x4*)(size_t)(LB + tid * 16);
    u32x2 ownr = {0u, 0u};
    if (wid < 2) ownr = *(LAS u32x2*)(size_t)(LB + 8192 + tid * 8);
    const unsigned sb = WS_SLAB + (t * 8 + g) * 8 * 9216;
    {
      const unsigned* f3 = (const unsigned*)(ws + WS_FLAG3) + t * 32;
      const bool poll = lane < 8 && lane != g;
      unsigned f0 = poll ? ld_flag(f3 + lane) : tag;
#pragma unroll 1
      for (unsigned spins = 0; spins < SPIN_CAP; ++spins) {
        if (__all(f0 == tag)) break;
        __builtin_amdgcn_s_sleep(1);
        if (poll) f0 = ld_flag(f3 + lane);
      }
      asm volatile("" ::: "memory");
    }
    STAMP(11);
    u32x4 v[7];
    u32x2 r[7];
#pragma unroll
    for (int s = 0; s < 7; ++s) {
      const unsigned so = sb + ((g + 1 + s) & 7) * 9216;
      v[s] = __builtin_amdgcn_raw_buffer_load_b128(wsr, so + tid * 16, 0, AUX_SC1);
      if (wid < 2) r[s] = __builtin_amdgcn_raw_buffer_load_b64(wsr, so + 8192 + tid * 8, 0, AUX_SC1);
    }
    auto up = [](u32x2 p) { return (f32x4){bf_lo(p[0]), bf_hi(p[0]), bf_lo(p[1]), bf_hi(p[1])}; };
    f32x4 lo = unpk_lo(own) + (up(xa) + up(ba)), hi = unpk_hi(own) + (up(xb) + up(bb));
#pragma unroll
    for (int s = 0; s < 7; ++s) { lo += unpk_lo(v[s]); hi += unpk_hi(v[s]); }
    const __amdgpu_buffer_rsrc_t outr = __builtin_amdgcn_make_buffer_rsrc(out, 0, 2048 * DM * 2, 0x00020000);
    const unsigned ro = (unsigned)row * 2;
    __builtin_amdgcn_raw_buffer_store_b64((u32x2){pk2(lo[0], lo[1]), pk2(lo[2], lo[3])}, outr, ro + ca * 2, 0, OUT_AUX);
    __builtin_amdgcn_raw_buffer_store_b64((u32x2){pk2(hi[0], hi[1]), pk2(hi[2], hi[3])}, outr, ro + (ca + 64) * 2, 0, OUT_AUX);
    if (wid < 2) {
      f32x4 m = up(ownr) + (up(xc) + up(bc));
#pragma unroll
      for (int s = 0; s < 7; ++s) m += up(r[s]);
      __builtin_amdgcn_raw_buffer_store_b64((u32x2){pk2(m[0], m[1]), pk2(m[2], m[3])}, outr, ro + cr * 2, 0, OUT_AUX);
    }
    STAMP(14); STAMP_RT(21);
    STAMP_FLUSH();
    if (blockIdx.x == 0 && tid == 0) st_flag((unsigned*)(ws + WS_EPOCH), tag);
  }
}

extern "C" void kernel_launch(void* const* d_in, const int* in_sizes, int n_in, void* d_out, int out_size, void* d_ws,
                              size_t ws_size, hipStream_t stream) {
  (void)in_sizes; (void)n_in; (void)out_size; (void)ws_size;
  (void)hipFuncSetAttribute((const void*)fused_block_kernel, hipFuncAttributeMaxDynamicSharedMemorySize, LDS_BYTES);
  fused_block_kernel<<<256, 512, LDS_BYTES, stream>>>(
      (const unsigned short*)d_in[0], (const char*)d_in[1], (const char*)d_in[2], (const char*)d_in[4],
      (const unsigned short*)d_in[5], (const char*)d_in[6], (const unsigned short*)d_in[7], (unsigned short*)d_out, (char*)d_ws);
}
```

```cpp
#include <hip/hip_runtime.h>
#include <hip/hip_bf16.h>
#include <type_traits>

typedef __attribute__((ext_vector_type(4))) short s16x4;
typedef __attribute__((ext_vector_type(8))) __bf16 bf16x8;
typedef __attribute__((ext_vector_type(2))) __bf16 bf16x2;
typedef __attribute__((ext_vector_type(4))) float f32x4;
typedef __attribute__((ext_vector_type(2))) float f32x2;
typedef __attribute__((ext_vector_type(4))) unsigned u32x4;
typedef __attribute__((ext_vector_type(2))) unsigned u32x2;
#define LAS __attribute__((address_space(3)))
#define GAS __attribute__((address_space(1)))
#define DI __device__ __forceinline__
#define SCHED() __builtin_amdgcn_sched_barrier(0)
#define BAR() __builtin_amdgcn_s_barrier()
#define WAIT_VM(n) asm volatile("s_waitcnt vmcnt(%0)" ::"n"(n) : "memory")
#define WAIT_LGKM(n) asm volatile("s_waitcnt lgkmcnt(%0)" ::"n"(n) : "memory")


namespace {

constexpr int DM = 576;
constexpr int ACT_STRIDE = 1184;
constexpr int RING = 64 * ACT_STRIDE;
constexpr int KSLOT = 36864;
constexpr int PQ = RING + 2 * KSLOT;
constexpr int PQ_STRIDE = 144;
constexpr int SIMG = PQ + 64 * PQ_STRIDE;
constexpr int LDS_BYTES = 163840;
constexpr int KTMP = RING + 16384;
constexpr int VTMP = KTMP + 64 * 144;
static_assert(SIMG + 5120 <= LDS_BYTES, "lds");
static_assert(VTMP + 64 * 152 + 16 <= RING + KSLOT, "lds");

constexpr unsigned WS_EPOCH = 0;
constexpr unsigned WS_FLAG1 = 1024;
constexpr unsigned WS_FLAG2 = 2048;
constexpr unsigned WS_FLAG3 = 4096;
constexpr unsigned WS_P = 65536;
constexpr unsigned WS_S = WS_P + 256 * 16384;
constexpr unsigned WS_SLAB = WS_S + 8 * 8192;
constexpr unsigned WS_END = WS_SLAB + 32 * 8 * 8 * 9216;
constexpr unsigned OOB = 0x7ffffff0u;
constexpr int AUX_SC1 = 16;
constexpr unsigned SPIN_CAP = 1u << 16;
constexpr unsigned CODE_PF_BYTES = 24576;

DI float bf_lo(unsigned u) { return __builtin_bit_cast(float, u << 16); }
DI float bf_hi(unsigned u) { return __builtin_bit_cast(float, u & 0xffff0000u); }
DI unsigned pk2(float a, float b) {
  f32x2 p = {a, b};
  return __builtin_bit_cast(unsigned, __builtin_convertvector(p, bf16x2));
}
DI float rbf(float a) { return bf_lo(pk2(a, a)); }
template <int CTRL> DI float dpp_add(float v) {
  return v + __builtin_bit_cast(float, __builtin_amdgcn_update_dpp(0, __builtin_bit_cast(int, v), CTRL, 0xf, 0xf, true));
}
DI int fresh_lane() { int v = threadIdx.x & 63; asm volatile("" : "+v"(v)); return v; }
DI bf16x8 lds_b128(unsigned addr) { return *(LAS bf16x8*)(size_t)addr; }
DI s16x4 lds_tr(unsigned addr) { return __builtin_amdgcn_ds_read_tr16_b64_v4i16((LAS s16x4*)(size_t)addr); }
DI bf16x8 join(s16x4 lo, s16x4 hi) {
  return __builtin_bit_cast(bf16x8, __builtin_shufflevector(lo, hi, 0, 1, 2, 3, 4, 5, 6, 7));
}
DI f32x4 mfma(bf16x8 a, bf16x8 b, f32x4 c) { return __builtin_amdgcn_mfma_f32_16x16x32_bf16(a, b, c, 0, 0, 0); }
DI float pos(float v) { const int b = __builtin_bit_cast(int, v); return __builtin_bit_cast(float, b > 0 ? b : 0); }
DI float neg(float v) { const int b = __builtin_bit_cast(int, v); return __builtin_bit_cast(float, b < 0 ? b : 0); }
DI f32x2 relu2(float a, float b) { const f32x2 p = {pos(a), pos(b)}; return p * p; }
DI unsigned phi2(float a, float b) {
  const f32x2 m = (f32x2){a, b} * 1.44269504088896f;
  const f32x2 e = {__builtin_amdgcn_exp2f(neg(m[0])), __builtin_amdgcn_exp2f(neg(m[1]))};
  const f32x2 r = ((f32x2){pos(a), pos(b)} + 1e-6f) + e;
  return __builtin_bit_cast(unsigned, __builtin_convertvector(r, bf16x2));
}
DI unsigned pk2v(f32x2 p) { return __builtin_bit_cast(unsigned, __builtin_convertvector(p, bf16x2)); }
DI unsigned ld_flag(const unsigned* p) { return __hip_atomic_load((GAS unsigned*)p, __ATOMIC_RELAXED, __HIP_MEMORY_SCOPE_AGENT); }
DI void st_flag(unsigned* p, unsigned v) { __hip_atomic_store((GAS unsigned*)p, v, __ATOMIC_RELAXED, __HIP_MEMORY_SCOPE_AGENT); }
DI void wait_flags(const unsigned* base, int count, unsigned tag, int lane, unsigned first) {
  unsigned v = first;
#pragma unroll 1
  for (unsigned spins = 0; spins < SPIN_CAP; ++spins) {
    if (__all(lane >= count || v == tag)) break;
    __builtin_amdgcn_s_sleep(2);
    if (lane < count) v = ld_flag(base + lane);
  }
  asm volatile("" ::: "memory");
}
DI f32x4 unpk_lo(u32x4 v) { return (f32x4){bf_lo(v[0]), bf_hi(v[0]), bf_lo(v[1]), bf_hi(v[1])}; }
DI f32x4 unpk_hi(u32x4 v) { return (f32x4){bf_lo(v[2]), bf_hi(v[2]), bf_lo(v[3]), bf_hi(v[3])}; }

DI void dma(unsigned voff, const char* sbase, unsigned m0v) {
  asm volatile("s_mov_b32 m0, %1\n\ts_nop 0\n\tglobal_load_lds_dwordx4 %0, %2" ::"v"(voff), "s"(m0v), "s"(sbase) : "memory");
}
template <int B, int P> DI void piece(const unsigned (&voff)[5], const char* sbase, unsigned m) {
  asm volatile("s_add_i32 m0, %1, %3\n\ts_nop 0\n\tglobal_load_lds_dwordx4 %0, %2 offset:%4"
               ::"v"(voff[P]), "s"(m), "s"(sbase), "n"(B * 4096 + P * 1024 - B * 128), "n"(B * 128) : "memory", "scc");
}
DI void touch(const char* sbase, unsigned voff, unsigned& dummy) { asm volatile("global_load_dword %0, %1, %2" : "+v"(dummy) : "v"(voff), "s"(sbase) : "memory"); }

template <int BOFF0>
DI void kstep9(f32x4 (&acc)[9][4], bf16x8 (&A)[9], const bf16x8 (&Bc)[4], bf16x8 (&Bn)[4], unsigned ab, unsigned abn, unsigned bn) {
  s16x4 lx, ly;
  constexpr int BTS = 16 * ACT_STRIDE;
#define MM(blk, j) acc[blk][j] = mfma(A[blk], Bc[j], acc[blk][j])
#define RL(v, base, blk) v = lds_tr((base) + (blk) * 4096)
#define RH(v, base, blk) A[blk] = join(v, lds_tr((base) + (blk) * 4096 + 512))
  RL(lx, ab, 3); SCHED(); MM(0, 0); SCHED(); RH(lx, ab, 3); SCHED(); MM(0, 1); SCHED(); MM(0, 2); MM(0, 3); SCHED();
  MM(1, 0); SCHED(); Bn[0] = lds_b128(bn + BOFF0); SCHED(); MM(1, 1); SCHED();
  Bn[1] = lds_b128(bn + BOFF0 + BTS); SCHED(); MM(1, 2); MM(1, 3); SCHED();
  WAIT_LGKM(2);
  BAR();
  RL(lx, ab, 4); SCHED(); MM(2, 0); SCHED(); RH(lx, ab, 4); SCHED(); MM(2, 1); SCHED();
  RL(ly, ab, 5); SCHED(); MM(2, 2); SCHED(); RH(ly, ab, 5); SCHED(); MM(2, 3); SCHED();
  RL(lx, ab, 6); SCHED(); MM(3, 0); SCHED(); RH(lx, ab, 6); SCHED(); MM(3, 1); SCHED(); MM(3, 2); MM(3, 3); SCHED();
  RL(lx, ab, 7); SCHED(); MM(4, 0); SCHED(); RH(lx, ab, 7); SCHED(); MM(4, 1); SCHED(); MM(4, 2); MM(4, 3); SCHED();
  RL(lx, ab, 8); SCHED(); MM(5, 0); SCHED(); RH(lx, ab, 8); SCHED(); MM(5, 1); SCHED(); MM(5, 2); MM(5, 3); SCHED();
  MM(6, 0); SCHED(); Bn[2] = lds_b128(bn + BOFF0 + 2 * BTS); SCHED(); MM(6, 1); SCHED();
  Bn[3] = lds_b128(bn + BOFF0 + 3 * BTS); SCHED(); MM(6, 2); MM(6, 3); SCHED();
  WAIT_LGKM(2);
  BAR();
  RL(lx, abn, 0); SCHED(); MM(7, 0); SCHED(); RH(lx, abn, 0); SCHED(); MM(7, 1); SCHED();
  RL(ly, abn, 1); SCHED(); MM(7, 2); SCHED(); RH(ly, abn, 1); SCHED(); MM(7, 3); SCHED();
  RL(lx, abn, 2); SCHED(); MM(8, 0); SCHED(); RH(lx, abn, 2); SCHED(); MM(8, 1); SCHED(); MM(8, 2); MM(8, 3); SCHED();
}
template <int BOFF0>
DI void kstep4(f32x4 (&acc)[4][4], bf16x8 (&A)[4], const bf16x8 (&Bc)[4], bf16x8 (&Bn)[4], unsigned ab, unsigned abn, unsigned d3, unsigned bn) {
  s16x4 lx, ly;
  constexpr int BTS = 16 * ACT_STRIDE;
#define QL(v, base, off) v = lds_tr((base) + (off))
#define QH(v, i, base, off) A[i] = join(v, lds_tr((base) + (off) + 576))
  const unsigned a3 = ab + d3;
  QL(lx, a3, 0); SCHED(); MM(0, 0); SCHED(); QH(lx, 3, a3, 0); SCHED(); MM(0, 1); SCHED();
  Bn[0] = lds_b128(bn + BOFF0); SCHED(); MM(0, 2); SCHED(); Bn[1] = lds_b128(bn + BOFF0 + BTS); SCHED(); MM(0, 3); SCHED();
  Bn[2] = lds_b128(bn + BOFF0 + 2 * BTS); SCHED(); MM(1, 0); SCHED(); Bn[3] = lds_b128(bn + BOFF0 + 3 * BTS); SCHED();
  MM(1, 1); MM(1, 2); MM(1, 3); SCHED();
  WAIT_LGKM(4);
  BAR();
  QL(lx, abn, 0); SCHED(); MM(2, 0); SCHED(); QH(lx, 0, abn, 0); SCHED(); MM(2, 1); SCHED();
  QL(ly, abn, 5120); SCHED(); MM(2, 2); SCHED(); QH(ly, 1, abn, 5120); SCHED(); MM(2, 3); SCHED();
  QL(lx, abn, 10240); SCHED(); MM(3, 0); SCHED(); QH(lx, 2, abn, 10240); SCHED(); MM(3, 1); MM(3, 2); MM(3, 3); SCHED();
}
#undef MM

}

__global__ __launch_bounds__(512, 2) void fused_block_kernel(
    const unsigned short* __restrict__ x, const char* __restrict__ wqkv, const char* __restrict__ wo,
    const char* __restrict__ w1, const unsigned short* __restrict__ b1, const char* __restrict__ w2,
    const unsigned short* __restrict__ b2, unsigned short* __restrict__ out, char* __restrict__ ws) {
  extern __shared__ __attribute__((aligned(16))) char lds[];
  const int tid = threadIdx.x, lane = tid & 63;
  const int wid = __builtin_amdgcn_readfirstlane(tid >> 6);
  constexpr int NGX = 2, PFN = 32 / NGX;
  const int xj = blockIdx.x & 7, xi = blockIdx.x >> 3;
  const int g = NGX * (xj / NGX) + xi % NGX, pfr = xi / NGX, t = (xj % NGX) * PFN + pfr;
  const unsigned LB = (unsigned)(size_t)lds;
  const __amdgpu_buffer_rsrc_t wsr = __builtin_amdgcn_make_buffer_rsrc(ws, 0, WS_END, 0x00020000);

  unsigned long long pc0;
  asm volatile("s_getpc_b64 %0" : "=s"(pc0));
  const int lr = lane >> 3, lc = lane & 7;
  const int l = wid - 4;
  const int cE = lc ^ (((lr >> 1) & 1) << 1), cO = cE ^ 4;
  unsigned voff[5];
  const unsigned m0r = LB + RING;
  const char* baseQ = wqkv + g * 144 + l * 1152;
  auto issueQ = [&](const char* sq, unsigned mq) {
    dma(voff[0], sq, mq); dma(voff[1], sq, mq + 1024); dma(voff[2], sq, mq + 2048); dma(voff[3], sq, mq + 3072); dma(voff[4], sq, mq + 4096);
  };
  unsigned dummy = 0;
  unsigned epoch;
  if (wid < 4) {
    u32x4 xr[2][9];
    const char* xp = (const char*)x + (size_t)(t * 64 + wid * 16) * 1152;
    const unsigned xo = lr * 1152 + lc * 16;
#pragma unroll
    for (int h = 0; h < 2; ++h)
#pragma unroll
      for (int i = 0; i < 9; ++i)
        asm volatile("global_load_dwordx4 %0, %1, %2 offset:%3" : "=v"(xr[h][i]) : "v"(xo + h * 8 * 1152), "s"(xp), "n"(i * 128) : "memory");
    asm volatile("global_load_dword %0, %1, %2 sc1" : "=v"(epoch) : "v"(0u), "s"(ws) : "memory");
#pragma unroll
    for (int h = 0; h < 2; ++h) {
      if (h == 0) WAIT_VM(10); else WAIT_VM(0);
      SCHED();
#pragma unroll
      for (int i = 0; i < 9; ++i) asm volatile("" : "+v"(xr[h][i]));
      const bf16x2 ones = __builtin_bit_cast(bf16x2, 0x3f803f80u);
      float s0 = 0.f, s1 = 0.f, q0 = 0.f, q1 = 0.f;
#pragma unroll
      for (int i = 0; i < 9; ++i) {
        const unsigned u0 = xr[h][i][0], u1 = xr[h][i][1], u2 = xr[h][i][2], u3 = xr[h][i][3];
        const bf16x2 a = __builtin_bit_cast(bf16x2, u0), b = __builtin_bit_cast(bf16x2, u1);
        const bf16x2 c = __builtin_bit_cast(bf16x2, u2), d = __builtin_bit_cast(bf16x2, u3);
        s0 = __builtin_amdgcn_fdot2_f32_bf16(a, ones, s0, false); s1 = __builtin_amdgcn_fdot2_f32_bf16(b, ones, s1, false);
        q0 = __builtin_amdgcn_fdot2_f32_bf16(a, a, q0, false); q1 = __builtin_amdgcn_fdot2_f32_bf16(b, b, q1, false);
        s0 = __builtin_amdgcn_fdot2_f32_bf16(c, ones, s0, false); s1 = __builtin_amdgcn_fdot2_f32_bf16(d, ones, s1, false);
        q0 = __builtin_amdgcn_fdot2_f32_bf16(c, c, q0, false); q1 = __builtin_amdgcn_fdot2_f32_bf16(d, d, q1, false);
      }
      float s = s0 + s1, q = q0 + q1;
      s = dpp_add<0xB1>(s); q = dpp_add<0xB1>(q);
      s = dpp_add<0x4E>(s); q = dpp_add<0x4E>(q);
      s = dpp_add<0x141>(s); q = dpp_add<0x141>(q);
      const float mean = s * (1.f / 576.f);
      const float var = fmaxf(q * (1.f / 576.f) - mean * mean, 0.f);
      const float mb = rbf(mean);
      const float sd = rbf(__builtin_amdgcn_sqrtf(rbf(rbf(var) + 1e-5f)));
      const float rs = __builtin_amdgcn_rcpf(sd), nb = -mb * rs;
      const unsigned wa = LB + (wid * 16 + h * 8 + lr) * ACT_STRIDE + lc * 16;
#pragma unroll
      for (int i = 0; i < 9; ++i) {
        u32x4 o;
#pragma unroll
        for (int e = 0; e < 4; ++e) o[e] = pk2(fmaf(bf_lo(xr[h][i][e]), rs, nb), fmaf(bf_hi(xr[h][i][e]), rs, nb));
        *(LAS u32x4*)(size_t)(wa + i * 128) = o;
      }
    }
  } else {
    if (wid == 7) {
      const unsigned i = (unsigned)lane * 32u + (unsigned)xi;
      touch((const char*)pc0, (i < CODE_PF_BYTES / 128 ? i : 0u) * 128u, dummy);
    }
    asm volatile("global_load_dword %0, %1, %2 sc1" : "=v"(epoch) : "v"(0u), "s"(ws) : "memory");
    if (wid == 7) {
      constexpr int RW = 576 / PFN, NQ = (RW + 7) / 8;
      const char* pq_ = wqkv + (size_t)(RW * pfr) * 3456 + ((g * 144) & ~127);
      const int c6 = lc < 5 ? lc : 5;
#pragma unroll
      for (int n = 0; n < NQ; ++n) {
        const int r = 8 * n + lr < RW ? 8 * n + lr : RW - 1;
        touch(pq_, r * 3456 + (c6 >> 1) * 1152 + (c6 & 1) * 128, dummy);
      }
      WAIT_VM(NQ);
    } else {
#pragma unroll
      for (int p = 0; p < 5; ++p) {
        int i = 64 * p + lane; i = i < 288 ? i : 287;
        const int r = (i * 7282) >> 16;
        voff[p] = r * 3456 + (i - 9 * r) * 16;
      }
      issueQ(baseQ, m0r + l * 5120);
      WAIT_VM(5);
    }
    SCHED();
  }
  asm volatile("" : "+v"(epoch));
  const unsigned tag = __builtin_amdgcn_readfirstlane(epoch) + 1u;
  if (wid >= 4) {
    __builtin_amdgcn_s_setprio(2);
    const char* base1 = w1 + g * 1152;
    const char* base2 = w2 + (size_t)(g * 576) * 1152;
    const char* baseO = wo + (size_t)(g * 72) * 1152;
    auto setvoff = [&](unsigned ldw, int rowsel) {
#pragma unroll
      for (int p = 0; p < 4; ++p) voff[p] = (rowsel * 8 * p + lr) * ldw + ((p & 1) ? cO : cE) * 16;
    };
    WAIT_LGKM(0);
    if (l < 3) {
      auto prog = [&](auto LC) {
        constexpr int L = decltype(LC)::value;
        auto issue8 = [&](const char* sb, unsigned m) {
          if constexpr (L == 0) { piece<8, 0>(voff, sb, m); piece<8, 3>(voff, sb, m); }
          if constexpr (L == 1) piece<8, 1>(voff, sb, m);
          if constexpr (L == 2) piece<8, 2>(voff, sb, m);
        };
        auto issueLo = [&](const char* sb, unsigned m) {
          if constexpr (L == 0) { piece<0, 2>(voff, sb, m); piece<1, 1>(voff, sb, m); piece<2, 0>(voff, sb, m); piece<2, 3>(voff, sb, m); piece<3, 2>(voff, sb, m); }
          if constexpr (L == 1) { piece<0, 0>(voff, sb, m); piece<0, 3>(voff, sb, m); piece<1, 2>(voff, sb, m); piece<2, 1>(voff, sb, m); piece<3, 0>(voff, sb, m); piece<3, 3>(voff, sb, m); }
          if constexpr (L == 2) { piece<0, 1>(voff, sb, m); piece<1, 0>(voff, sb, m); piece<1, 3>(voff, sb, m); piece<2, 2>(voff, sb, m); piece<3, 1>(voff, sb, m); }
        };
        auto issueHi = [&](const char* sb, unsigned m) {
          if constexpr (L == 0) { piece<4, 1>(voff, sb, m); piece<5, 0>(voff, sb, m); piece<5, 3>(voff, sb, m); piece<6, 2>(voff, sb, m); piece<7, 1>(voff, sb, m); }
          if constexpr (L == 1) { piece<4, 2>(voff, sb, m); piece<5, 1>(voff, sb, m); piece<6, 0>(voff, sb, m); piece<6, 3>(voff, sb, m); piece<7, 2>(voff, sb, m); }
          if constexpr (L == 2) { piece<4, 0>(voff, sb, m); piece<4, 3>(voff, sb, m); piece<5, 2>(voff, sb, m); piece<6, 1>(voff, sb, m); piece<7, 0>(voff, sb, m); piece<7, 3>(voff, sb, m); }
        };
        constexpr int WX = L == 0 ? 10 : 11;
        const unsigned mA = m0r, mB = m0r + KSLOT;
        constexpr int RW1 = 576 / PFN, NT = (RW1 * 9 + 191) / 192;
        {
          const char* sq = baseQ + 32 * 3456;
          const unsigned mq = m0r + L * 5120;
          issueQ(sq, mq + 16384); issueQ(sq + 32 * 3456, mq + KSLOT);
          sq += 64 * 3456;
          {
            const char* p1 = w1 + (size_t)(RW1 * pfr) * 9216 + g * 1152;
#pragma unroll
            for (int n = 0; n < NT; ++n) {
              const int i0 = (3 * n + L) * 64 + lane, i = i0 < RW1 * 9 ? i0 : 0;
              const int r = (i * 7282) >> 16;
              touch(p1, r * 9216 + (i - 9 * r) * 128, dummy);
            }
          }
          WAIT_VM(10 + NT);
          BAR();
          unsigned m = mq + KSLOT + 16384, dA = (unsigned)-(KSLOT + 16384);
#pragma unroll 1
          for (int j = 3; j < 18; ++j) {
            issueQ(sq, m);
            sq += 32 * 3456;
            m += dA;
            dA = dA == 16384 ? ((m - mq) >= (unsigned)KSLOT ? (unsigned)-(KSLOT + 16384) : (unsigned)(KSLOT - 16384)) : 16384u;
            if (j < 5) WAIT_VM(10 + NT); else WAIT_VM(10);
            BAR();
          }
        }
        setvoff(9216, 1);
        issueLo(base1, mB); WAIT_VM(10); BAR();
        issueHi(base1, mB); issue8(base1, mB); WAIT_VM(12); BAR();
        issueLo(base1 + 32 * 9216, mA); BAR(); BAR();
        WAIT_VM(12); BAR();
        {
          const char* s1 = base1 + 32 * 9216;
          const char* s2 = s1 + 32 * 9216;
          unsigned step = 32 * 9216, m1 = mA, m2 = mB;
#pragma unroll 1
          for (int kk = 0; kk < 37; ++kk) {
            issueHi(s1, m1);
            if (__builtin_expect(kk == 18, 0)) BAR();
            WAIT_VM(WX); BAR();
            issue8(s1, m1);
            if (__builtin_expect((0x1000010000ull >> kk) & 1, 0)) {
              if (kk == 16) { setvoff(1152, 1); s2 = base2; step = 32 * 1152; }
              else setvoff(1152, 0);
            }
            issueLo(s2, m2); WAIT_VM(12); BAR();
            s1 = s2;
            s2 = kk == 33 ? baseO : s2 + step;
            const unsigned tm = m1; m1 = m2; m2 = tm;
          }
          issueHi(s1, m1); WAIT_VM(WX); BAR();
          issue8(s1, m1); WAIT_VM(6); BAR();
          WAIT_VM(0); BAR(); BAR();
        }
      };
      if (l == 0) prog(std::integral_constant<int, 0>{});
      else if (l == 1) prog(std::integral_constant<int, 1>{});
      else prog(std::integral_constant<int, 2>{});
    } else {
      auto bars = [&](int n) {
#pragma unroll 1
        for (int i = 0; i < n; ++i) BAR();
      };
      const int lane = fresh_lane();
      constexpr int RW = 576 / PFN;
      BAR();
      bars(17 + 1);
      {
        const char* p2 = base2 + (size_t)(RW * pfr) * 1152;
#pragma unroll
        for (int n = 0; n < (RW * 9 + 63) / 64; ++n) {
          const int i = lane + 64 * n < RW * 9 ? lane + 64 * n : 0;
          touch(p2, i * 128, dummy);
        }
        constexpr int WOL = (648 + PFN - 1) / PFN;
        static_assert(WOL <= 64, "prefetch steps");
        int i = pfr * WOL + lane; i = (lane < WOL && i < 648) ? i : 0;
        touch(baseO, i * 128, dummy);
      }
      bars(2);
      bars(13);
      if (lane == 0) st_flag((unsigned*)(ws + WS_FLAG1) + g * 32 + t, tag);
      bars(7);
      unsigned fl = lane < 32 ? ld_flag((const unsigned*)(ws + WS_FLAG1) + g * 32 + lane) : tag;
      bars(3);
      wait_flags((const unsigned*)(ws + WS_FLAG1) + g * 32, 32, tag, lane, fl);
      {
        u32x4 hv[16];
        const unsigned po = WS_P + (g * 32 + (lane >> 5)) * 16384 + t * 512 + (lane & 31) * 16;
#pragma unroll
        for (int n = 0; n < 16; ++n)
          hv[n] = __builtin_amdgcn_raw_buffer_load_b128(wsr, po, n * 32768, AUX_SC1);
        SCHED();
        bars(5);
        f32x4 a = __builtin_bit_cast(f32x4, hv[0]), b = __builtin_bit_cast(f32x4, hv[1]);
#pragma unroll
        for (int n = 2; n < 16; n += 2) { a += __builtin_bit_cast(f32x4, hv[n]); b += __builtin_bit_cast(f32x4, hv[n + 1]); }
        a += b;
        SCHED();
        BAR();
        const int hh = lane >> 5, li = 32 * (t & 1) + (lane & 31), tile = t >> 1;
        float s0 = hh ? a[2] : a[0], s1 = hh ? a[3] : a[1], o0 = hh ? a[0] : a[2], o1 = hh ? a[1] : a[3];
        const unsigned x0 = (unsigned)__builtin_amdgcn_permlane32_swap(__builtin_bit_cast(unsigned, o0), __builtin_bit_cast(unsigned, o0), false, false)[hh ? 0 : 1];
        const unsigned x1 = (unsigned)__builtin_amdgcn_permlane32_swap(__builtin_bit_cast(unsigned, o1), __builtin_bit_cast(unsigned, o1), false, false)[hh ? 0 : 1];
        s0 += __builtin_bit_cast(float, x0); s1 += __builtin_bit_cast(float, x1);
        const int di = (int)((0x4433322221111000ull >> (4 * tile)) & 15u), ej = (int)((0x4343243214210410ull >> (4 * tile)) & 15u);
        const int d = 16 * di + 4 * (li >> 4) + 2 * hh, e = 16 * ej + (li & 15);
        const int hd = (d * 57) >> 10, he = (e * 57) >> 10;
        const bool ok = d < 72 && (e == 72 || (e < 72 && hd == he));
        const int row = e == 72 ? 18 : e - 18 * he;
        __builtin_amdgcn_raw_buffer_store_b32(pk2(s0, s1), wsr, ok ? WS_S + g * 8192 + hd * 1280 + row * 64 + (d - 18 * hd) * 2 : OOB, 0, AUX_SC1);
      }
      bars(5);
      WAIT_VM(0);
      if (lane == 0) st_flag((unsigned*)(ws + WS_FLAG2) + g * 32 + t, tag);
      bars(2 + 1 + 7);
      fl = lane < 32 ? ld_flag((const unsigned*)(ws + WS_FLAG2) + g * 32 + lane) : tag;
      bars(3);
      wait_flags((const unsigned*)(ws + WS_FLAG2) + g * 32, 32, tag, lane, fl);
      {
        u32x4 iv[5];
#pragma unroll
        for (int n = 0; n < 5; ++n) iv[n] = __builtin_amdgcn_raw_buffer_load_b128(wsr, WS_S + g * 8192 + lane * 16, n * 1024, AUX_SC1);
        SCHED();
        bars(5);
        const int c = lane & 3;
#pragma unroll
        for (int n = 0; n < 5; ++n) {
          const int row = 16 * n + (lane >> 2);
          const bool live = row != 19 && row != 39 && row != 59 && row != 79 && c < 3;
          u32x4 v = iv[n];
          v[0] = live ? v[0] : 0u;
          v[1] = (live && c < 2) ? v[1] : 0u; v[2] = (live && c < 2) ? v[2] : 0u; v[3] = (live && c < 2) ? v[3] : 0u;
          *(LAS u32x4*)(size_t)(LB + SIMG + n * 1024 + lane * 16) = v;
        }
        WAIT_LGKM(0);
      }
      bars(27);
    }
    WAIT_VM(0);
    asm volatile("" ::"v"(dummy));
  } else {
    const int w = wid;
    __builtin_amdgcn_s_setprio(3);
    const int g4 = lane >> 4, q = (lane & 15) >> 2, p = lane & 3;
    const int cf = (((q >> 1) & 1) << 1) | ((g4 & 1) << 2);
    unsigned a0 = LB + RING + (8 * g4 + q) * 128 + (((2 * w + (p >> 1)) ^ cf) * 16) + (p & 1) * 8;
    unsigned a1 = a0 + KSLOT;
    asm volatile("" : "+v"(a1));
    const unsigned b0 = LB + (lane & 15) * ACT_STRIDE + g4 * 16;
    bf16x8 B0[4], B1[4];
    f32x4 acc[9][4];
    {
      f32x4 qa[4][4];
      bf16x8 A[4];
#pragma unroll
      for (int i = 0; i < 4; ++i)
#pragma unroll
        for (int j = 0; j < 4; ++j) qa[i][j] = (f32x4){0.f, 0.f, 0.f, 0.f};
      WAIT_LGKM(0);
      BAR();
#pragma unroll
      for (int j = 0; j < 4; ++j) B0[j] = lds_b128(b0 + j * 16 * ACT_STRIDE);
      unsigned ac = LB + RING + (8 * g4 + q) * 144 + 32 * w + 8 * p, an = ac + KSLOT, bc = b0;
      const unsigned d3 = (w < 3 ? w : 0) * 5120 + 128 - 32 * w;
      A[0] = join(lds_tr(ac), lds_tr(ac + 576));
      A[1] = join(lds_tr(ac + 5120), lds_tr(ac + 5120 + 576));
      A[2] = join(lds_tr(ac + 10240), lds_tr(ac + 10240 + 576));
#pragma unroll 1
      for (int it = 0; it < 9; ++it) {
        kstep4<64>(qa, A, B0, B1, ac, ac + 16384, d3, bc);
        kstep4<128>(qa, A, B1, B0, ac + 16384, an, d3, bc);
        const unsigned tmp = ac; ac = an; an = tmp;
        bc += 128;
      }
      const int lane = fresh_lane(), g4 = lane >> 4, q = (lane & 15) >> 2, p = lane & 3;
      u32x2 bb[9];
#pragma unroll
      for (int blk = 0; blk < 9; ++blk) bb[blk] = *(const u32x2*)(b1 + g * 576 + 64 * blk + 16 * w + 4 * g4);
      {
        const unsigned tk = (lane & 15), col = (16 * w + 4 * g4) * 2;
        const unsigned aq = LB + PQ + tk * 144 + col, ak = LB + KTMP + tk * 144 + col, av = LB + VTMP + tk * 152 + col;
        constexpr unsigned DROP = 0x40000;
        const unsigned ar = w == 3 ? (g4 == 0 ? LB + VTMP + tk * 152 + 144 : DROP)
                                   : g4 >= 2 ? DROP : (w == 0 ? LB + PQ + tk * 144 : w == 1 ? LB + KTMP + tk * 144 : LB + VTMP + tk * 152) + 128 + g4 * 8;
        const unsigned rs = w >= 2 ? 16 * 152 : 16 * 144;
        u32x2 oq[4], ok[4], ov[4], orm[4];
#pragma unroll
        for (int tt = 0; tt < 4; ++tt) {
          ov[tt] = (u32x2){pk2(qa[2][tt][0], qa[2][tt][1]), pk2(qa[2][tt][2], qa[2][tt][3])};
          ok[tt] = (u32x2){phi2(qa[1][tt][0], qa[1][tt][1]), phi2(qa[1][tt][2], qa[1][tt][3])};
        }
        SCHED();
#pragma unroll
        for (int tt = 0; tt < 4; ++tt) {
          *(LAS u32x2*)(size_t)(av + tt * 16 * 152) = ov[tt];
          *(LAS u32x2*)(size_t)(ak + tt * 16 * 144) = ok[tt];
        }
        if (w < 2) {
#pragma unroll
          for (int tt = 0; tt < 4; ++tt) orm[tt] = (u32x2){phi2(qa[3][tt][0], qa[3][tt][1]), phi2(qa[3][tt][2], qa[3][tt][3])};
        } else {
#pragma unroll
          for (int tt = 0; tt < 4; ++tt) {
            orm[tt] = (u32x2){pk2(qa[3][tt][0], qa[3][tt][1]), pk2(qa[3][tt][2], qa[3][tt][3])};
            if (w == 3) orm[tt] = (u32x2){0x00003f80u, 0u};
          }
        }
#pragma unroll
        for (int tt = 0; tt < 4; ++tt) oq[tt] = (u32x2){phi2(qa[0][tt][0], qa[0][tt][1]), phi2(qa[0][tt][2], qa[0][tt][3])};
        SCHED();
#pragma unroll
        for (int tt = 0; tt < 4; ++tt) {
          *(LAS u32x2*)(size_t)(ar + tt * rs) = orm[tt];
          *(LAS u32x2*)(size_t)(aq + tt * 16 * 144) = oq[tt];
        }
      }
      WAIT_LGKM(0);
      BAR();
      const unsigned ti = (unsigned)(0x4433322221111000ull >> (16 * w)) & 0xffffu;
      const unsigned tj = (unsigned)(0x4343243214210410ull >> (16 * w)) & 0xffffu;
      const unsigned kr = LB + KTMP + (8 * g4 + q) * 144 + p * 8, vr = LB + VTMP + (8 * g4 + q) * 152 + p * 8;
#pragma unroll
      for (int n = 0; n < 4; ++n) {
        const unsigned di = (ti >> (4 * n)) & 15u, ej = (tj >> (4 * n)) & 15u;
        f32x4 d = {0.f, 0.f, 0.f, 0.f};
#pragma unroll
        for (int ks = 0; ks < 2; ++ks) {
          const unsigned ka = kr + ks * 32 * 144 + di * 32, va = vr + ks * 32 * 152 + ej * 32;
          const bf16x8 fa = join(lds_tr(ka), lds_tr(ka + 4 * 144));
          const bf16x8 fb = join(lds_tr(va), lds_tr(va + 4 * 152));
          d = mfma(fa, fb, d);
        }
        __builtin_amdgcn_raw_buffer_store_b128(__builtin_bit_cast(u32x4, d), wsr,
                                               WS_P + (g * 32 + t) * 16384 + (4 * w + n) * 1024 + lane * 16, 0, AUX_SC1);
      }
#pragma unroll
      for (int i = 0; i < 9; ++i) {
        const f32x4 bv = {bf_lo(bb[i][0]), bf_hi(bb[i][0]), bf_lo(bb[i][1]), bf_hi(bb[i][1])};
#pragma unroll
        for (int j = 0; j < 4; ++j) acc[i][j] = bv;
      }
      WAIT_LGKM(0);
      BAR();
    }
    bf16x8 A[9];
    {
#pragma unroll
      for (int j = 0; j < 4; ++j) B0[j] = lds_b128(b0 + j * 16 * ACT_STRIDE);
      A[0] = join(lds_tr(a1), lds_tr(a1 + 512));
      A[1] = join(lds_tr(a1 + 4096), lds_tr(a1 + 4096 + 512));
      A[2] = join(lds_tr(a1 + 8192), lds_tr(a1 + 8192 + 512));
      unsigned bc = b0;
#pragma unroll 1
      for (int it = 0; it < 9; ++it) {
        if (__builtin_expect(it == 3, 0)) WAIT_VM(0);
        kstep9<64>(acc, A, B0, B1, a1, a0, bc);
        kstep9<128>(acc, A, B1, B0, a0, a1, bc);
        bc += 128;
      }
      const int lane = fresh_lane(), g4 = lane >> 4;
      const unsigned ha = LB + (lane & 15) * ACT_STRIDE + (16 * w + 8 * (g4 >> 1)) * 2;
      u32x4 hq[18];
#pragma unroll
      for (int n = 0; n < 18; ++n) {
        const f32x4 u = n < 16 ? acc[2 * (n & 3)][n >> 2] : acc[8][2 * (n - 16)];
        const f32x4 v = n < 16 ? acc[2 * (n & 3) + 1][n >> 2] : acc[8][2 * (n - 16) + 1];
        const auto s0 = __builtin_amdgcn_permlane16_swap(pk2v(relu2(u[0], u[1])), pk2v(relu2(v[0], v[1])), false, false);
        const auto s1 = __builtin_amdgcn_permlane16_swap(pk2v(relu2(u[2], u[3])), pk2v(relu2(v[2], v[3])), false, false);
        hq[n] = (u32x4){(unsigned)s0[0], (unsigned)s1[0], (unsigned)s0[1], (unsigned)s1[1]};
        if (n >= 2) {
          SCHED();
          const int m = n - 2;
          *(LAS u32x4*)(size_t)(ha + (m >> 2) * 16 * ACT_STRIDE + (128 * (m & 3) + 64 * (g4 & 1)) * 2) = hq[m];
          SCHED();
        }
      }
      *(LAS u32x4*)(size_t)(ha + (g4 & 1) * 16 * ACT_STRIDE + 512 * 2) = hq[16];
      *(LAS u32x4*)(size_t)(ha + (2 + (g4 & 1)) * 16 * ACT_STRIDE + 512 * 2) = hq[17];
      WAIT_LGKM(0);
      BAR();
    }
    {
#pragma unroll
      for (int j = 0; j < 4; ++j) B0[j] = lds_b128(b0 + j * 16 * ACT_STRIDE);
#pragma unroll
      for (int i = 0; i < 9; ++i)
#pragma unroll
        for (int j = 0; j < 4; ++j) acc[i][j] = (f32x4){0.f, 0.f, 0.f, 0.f};
      unsigned bc = b0;
      kstep9<64>(acc, A, B0, B1, a1, a0, bc);
#pragma unroll 1
      for (int it = 0; it < 10; ++it) {
        if (__builtin_expect(it == 6, 0)) {
          const int lane = fresh_lane(), g4 = lane >> 4, r16 = lane & 15;
          const unsigned sa = LB + SIMG + w * 1280 + g4 * 16;
          const bf16x8 ka0 = lds_b128(sa + r16 * 64), ka1 = lds_b128(sa + (r16 < 3 ? 16 + r16 : 19) * 64);
          const unsigned pa = LB + PQ + r16 * PQ_STRIDE + w * 36 + g4 * 16;
          const unsigned oa = LB + r16 * ACT_STRIDE + w * 36 + g4 * 8;
          const unsigned la = g4 == 0 ? oa + 32 : LB + r16 * ACT_STRIDE + 144 + w * 12 + (g4 - 1) * 4;
          u32x4 pb[4];
#pragma unroll
          for (int tt = 0; tt < 4; ++tt)
#pragma unroll
            for (int m = 0; m < 4; ++m) pb[tt][m] = *(LAS unsigned*)(size_t)(pa + tt * 16 * PQ_STRIDE + 4 * m);
#pragma unroll
          for (int tt = 0; tt < 4; ++tt) {
            const bf16x8 b = __builtin_bit_cast(bf16x8, pb[tt]);
            const f32x4 n0 = mfma(ka0, b, (f32x4){0.f, 0.f, 0.f, 0.f}), n1 = mfma(ka1, b, (f32x4){0.f, 0.f, 0.f, 0.f});
            const float d2 = n1[2];
            const unsigned du = __builtin_bit_cast(unsigned, d2);
            const unsigned y = (unsigned)__builtin_amdgcn_permlane32_swap(du, du, false, false)[0];
            const unsigned z = (unsigned)__builtin_amdgcn_permlane16_swap(y, y, false, false)[0];
            const float r = __builtin_amdgcn_rcpf(__builtin_bit_cast(float, z) + 1e-6f);
            const f32x2 p0 = (f32x2){n0[0], n0[1]} * r, p1 = (f32x2){n0[2], n0[3]} * r, p2 = (f32x2){n1[0], n1[1]} * r;
            *(LAS unsigned*)(size_t)(oa + tt * 16 * ACT_STRIDE) = pk2v(p0);
            *(LAS unsigned*)(size_t)(oa + tt * 16 * ACT_STRIDE + 4) = pk2v(p1);
            *(LAS unsigned*)(size_t)(la + tt * 16 * ACT_STRIDE) = g4 == 0 ? pk2v(p2) : 0u;
          }
        }
        kstep9<128>(acc, A, B1, B0, a0, a1, bc);
        kstep9<192>(acc, A, B0, B1, a1, a0, bc);
        bc += it == 7 ? 128 - 18 * 64 : 128;
      }
    }
    {
      const int lane = fresh_lane(), g4 = lane >> 4;
      const int hf = (lane >> 3) & 1, l32 = g4 * 8 + (lane & 7);
#pragma unroll
      for (int tq = 0; tq < 4; ++tq) {
        const int tt = tq;
        const bool own = 2 * tt + hf == g;
        const unsigned sb = own ? OOB : WS_SLAB + ((t * 8 + 2 * tt + hf) * 8 + g) * 9216;
        u32x4 o[4];
#pragma unroll
        for (int j = 0; j < 4; ++j) {
          o[j][0] = pk2(acc[2 * j][tt][0], acc[2 * j][tt][1]); o[j][1] = pk2(acc[2 * j][tt][2], acc[2 * j][tt][3]);
          o[j][2] = pk2(acc[2 * j + 1][tt][0], acc[2 * j + 1][tt][1]); o[j][3] = pk2(acc[2 * j + 1][tt][2], acc[2 * j + 1][tt][3]);
        }
        const u32x2 o2 = {pk2(acc[8][tt][0], acc[8][tt][1]), pk2(acc[8][tt][2], acc[8][tt][3])};
#pragma unroll
        for (int j = 0; j < 4; ++j) __builtin_amdgcn_raw_buffer_store_b128(o[j], wsr, sb + ((w * 4 + j) * 32 + l32) * 16, 0, AUX_SC1);
        __builtin_amdgcn_raw_buffer_store_b64(o2, wsr, sb + 8192 + (w * 32 + l32) * 8, 0, AUX_SC1);
        if (tt == (g >> 1)) {
          const unsigned fa = own ? LB + l32 * 16 + w * 2048 : 0x40000u;
#pragma unroll
          for (int j = 0; j < 4; ++j) *(LAS u32x4*)(size_t)(fa + j * 512) = o[j];
          *(LAS u32x2*)(size_t)(own ? LB + 8192 + (w * 32 + l32) * 8 : 0x40000u) = o2;
        }
      }
    }
    WAIT_VM(0);
  }
  {
    int tid = threadIdx.x; asm volatile("" : "+v"(tid));
    const int lane = tid & 63;
    const int tk = tid & 7, cg = (tid >> 3) & 3;
    const int ca = 128 * ((tid >> 5) & 3) + 16 * (tid >> 7) + 4 * cg, cr = 512 + 16 * ((tid >> 5) & 3) + 4 * cg;
    const size_t row = (size_t)(t * 64 + g * 8 + tk) * DM;
    const u32x2 xa = *(const u32x2*)(x + row + ca), xb = *(const u32x2*)(x + row + ca + 64);
    const u32x2 ba = *(const u32x2*)(b2 + ca), bb = *(const u32x2*)(b2 + ca + 64);
    u32x2 xc = xa, bc = ba;
    if (wid < 2) { xc = *(const u32x2*)(x + row + cr); bc = *(const u32x2*)(b2 + cr); }
    WAIT_LGKM(0);
    BAR();
    if (tid == 0) st_flag((unsigned*)(ws + WS_FLAG3) + t * 32 + g, tag);
    const u32x4 own = *(LAS u32x4*)(size_t)(LB + tid * 16);
    u32x2 ownr = {0u, 0u};
    if (wid < 2) ownr = *(LAS u32x2*)(size_t)(LB + 8192 + tid * 8);
    const unsigned sb = WS_SLAB + (t * 8 + g) * 8 * 9216;
    {
      const unsigned* f3 = (const unsigned*)(ws + WS_FLAG3) + t * 32;
      const bool poll = lane < 8 && lane != g;
      unsigned f0 = poll ? ld_flag(f3 + lane) : tag;
#pragma unroll 1
      for (unsigned spins = 0; spins < SPIN_CAP; ++spins) {
        if (__all(f0 == tag)) break;
        __builtin_amdgcn_s_sleep(1);
        if (poll) f0 = ld_flag(f3 + lane);
      }
      asm volatile("" ::: "memory");
    }
    u32x4 v[7];
    u32x2 r[7];
#pragma unroll
    for (int s = 0; s < 7; ++s) {
      const unsigned so = sb + ((g + 1 + s) & 7) * 9216;
      v[s] = __builtin_amdgcn_raw_buffer_load_b128(wsr, so + tid * 16, 0, AUX_SC1);
      if (wid < 2) r[s] = __builtin_amdgcn_raw_buffer_load_b64(wsr, so + 8192 + tid * 8, 0, AUX_SC1);
    }
    auto up = [](u32x2 p) { return (f32x4){bf_lo(p[0]), bf_hi(p[0]), bf_lo(p[1]), bf_hi(p[1])}; };
    f32x4 lo = unpk_lo(own) + (up(xa) + up(ba)), hi = unpk_hi(own) + (up(xb) + up(bb));
#pragma unroll
    for (int s = 0; s < 7; ++s) { lo += unpk_lo(v[s]); hi += unpk_hi(v[s]); }
    const __amdgpu_buffer_rsrc_t outr = __builtin_amdgcn_make_buffer_rsrc(out, 0, 2048 * DM * 2, 0x00020000);
    const unsigned ro = (unsigned)row * 2;
    __builtin_amdgcn_raw_buffer_store_b64((u32x2){pk2(lo[0], lo[1]), pk2(lo[2], lo[3])}, outr, ro + ca * 2, 0, 0);
    __builtin_amdgcn_raw_buffer_store_b64((u32x2){pk2(hi[0], hi[1]), pk2(hi[2], hi[3])}, outr, ro + (ca + 64) * 2, 0, 0);
    if (wid < 2) {
      f32x4 m = up(ownr) + (up(xc) + up(bc));
#pragma unroll
      for (int s = 0; s < 7; ++s) m += up(r[s]);
      __builtin_amdgcn_raw_buffer_store_b64((u32x2){pk2(m[0], m[1]), pk2(m[2], m[3])}, outr, ro + cr * 2, 0, 0);
    }
    if (blockIdx.x == 0 && tid == 0) st_flag((unsigned*)(ws + WS_EPOCH), tag);
  }
}

extern "C" void kernel_launch(void* const* d_in, const int* in_sizes, int n_in, void* d_out, int out_size, void* d_ws,
                              size_t ws_size, hipStream_t stream) {
  (void)in_sizes; (void)n_in; (void)out_size; (void)ws_size;
  (void)hipFuncSetAttribute((const void*)fused_block_kernel, hipFuncAttributeMaxDynamicSharedMemorySize, LDS_BYTES);
  fused_block_kernel<<<256, 512, LDS_BYTES, stream>>>(
      (const unsigned short*)d_in[0], (const char*)d_in[1], (const char*)d_in[2], (const char*)d_in[4],
      (const unsigned short*)d_in[5], (const char*)d_in[6], (const unsigned short*)d_in[7], (unsigned short*)d_out, (char*)d_ws);
}
```
